```python
import torch

def _softmax(x, axis=-1):
    return torch.softmax(x, dim=axis)
def _gelu(x):
    return torch.nn.functional.gelu(x, approximate="tanh")
def _silu(x):
    return torch.nn.functional.silu(x)
def _sigmoid(x):
    return torch.sigmoid(x)
def _layernorm(x, axis=-1, eps=1e-5):
    m = x.mean(dim=axis, keepdim=True)
    v = x.var(dim=axis, keepdim=True, unbiased=False)
    return (x - m) / torch.sqrt(v + eps)
def _scatter_add(tgt, idx, src, axis=0):
    return tgt.clone().index_add_(axis, idx, src)
def _segment_sum(src, seg, n):
    out = torch.zeros((n,) + src.shape[1:], dtype=src.dtype)
    return out.index_add_(0, seg, src)
def _pad(x, pw):
    flat = tuple(v for pair in reversed(pw) for v in pair)
    return torch.nn.functional.pad(x, flat)

kernel_name = 'concat_heads__mha_block_cached__mha_block_cached__near_ridge'

def setup_inputs():

    _w = (lambda a: (lambda t: t.to(torch.bfloat16) if t.is_floating_point() else t)(torch.as_tensor(a)))
    def _A_setup():
        B, Sq, D = 8, 240, 640
        H, Sk, Dh = 32, 1024, 128
        return {
            "x": _w((lambda *s: torch.randn(*s, dtype=torch.bfloat16) * 0.8)(B, Sq, D)),
            "k_cache": _w((lambda *s: torch.randn(*s, dtype=torch.bfloat16) * 0.8)(B, H, Sk, Dh)),
            "v_cache": _w((lambda *s: torch.randn(*s, dtype=torch.bfloat16) * 0.05)(B, H, Sk, Dh)),
            "wq": _w((lambda *s: torch.randn(*s, dtype=torch.bfloat16) * 0.8)(D, H * Dh) * (D ** -0.5)),
            "wo": _w((lambda *s: torch.randn(*s, dtype=torch.bfloat16) * 0.05)(H * Dh, D) * ((H * Dh) ** -0.5)),
        }
    _A_in = _A_setup()

    def _B_setup():
        B, Sq, D = 8, 240, 640
        H, Sk, Dh = 32, 1024, 128
        return {
            "x": _w((lambda *s: torch.randn(*s, dtype=torch.bfloat16) * 0.8)(B, Sq, D)),
            "k_cache": _w((lambda *s: torch.randn(*s, dtype=torch.bfloat16) * 0.8)(B, H, Sk, Dh)),
            "v_cache": _w((lambda *s: torch.randn(*s, dtype=torch.bfloat16) * 0.05)(B, H, Sk, Dh)),
            "wq": _w((lambda *s: torch.randn(*s, dtype=torch.bfloat16) * 0.8)(D, H * Dh) * (D ** -0.5)),
            "wo": _w((lambda *s: torch.randn(*s, dtype=torch.bfloat16) * 0.05)(H * Dh, D) * ((H * Dh) ** -0.5)),
        }
    _B_in = _B_setup()

    return {
        "A_x": _A_in["x"],
        "A_k_cache": _A_in["k_cache"],
        "A_v_cache": _A_in["v_cache"],
        "A_wq": _A_in["wq"],
        "A_wo": _A_in["wo"],
        "B_x": _B_in["x"],
        "B_k_cache": _B_in["k_cache"],
        "B_v_cache": _B_in["v_cache"],
        "B_wq": _B_in["wq"],
        "B_wo": _B_in["wo"],
    }

def reference(A_x, A_k_cache, A_v_cache, A_wq, A_wo, B_x, B_k_cache, B_v_cache, B_wq, B_wo):
    def _A_ref(x, k_cache, v_cache, wq, wo):
        B, Sq, D = x.shape
        H, Dh = 32, 128
        q = (lambda x, *d: x.permute(*d))((x @ wq).reshape(B, Sq, H, Dh), 0, 2, 1, 3)
        scores = torch.einsum("bhqd,bhkd->bhqk", q, k_cache) * (Dh ** -0.5)
        p = _softmax(scores, axis=-1)
        o = torch.einsum("bhqk,bhkd->bhqd", p, v_cache)
        return (lambda x, *d: x.permute(*d))(o, 0, 2, 1, 3).reshape(B, Sq, H * Dh) @ wo

    def _B_ref(x, k_cache, v_cache, wq, wo):
        B, Sq, D = x.shape
        H, Dh = 32, 128
        q = (lambda x, *d: x.permute(*d))((x @ wq).reshape(B, Sq, H, Dh), 0, 2, 1, 3)
        scores = torch.einsum("bhqd,bhkd->bhqk", q, k_cache) * (Dh ** -0.5)
        p = _softmax(scores, axis=-1)
        o = torch.einsum("bhqk,bhkd->bhqd", p, v_cache)
        return (lambda x, *d: x.permute(*d))(o, 0, 2, 1, 3).reshape(B, Sq, H * Dh) @ wo

    a = _A_ref(A_x, A_k_cache, A_v_cache, A_wq, A_wo)
    b = _B_ref(B_x, B_k_cache, B_v_cache, B_wq, B_wo)
    return torch.concatenate([a, b], axis=-1)
```

```cpp
#include <hip/hip_runtime.h>
#include <hip/hip_bf16.h>
#include <stdint.h>

typedef short    bf16x8 __attribute__((ext_vector_type(8)));
typedef short    s16x4  __attribute__((ext_vector_type(4)));
typedef float    f32x16 __attribute__((ext_vector_type(16)));
typedef unsigned u32x4  __attribute__((ext_vector_type(4)));
typedef unsigned u32x2  __attribute__((ext_vector_type(2)));
typedef unsigned short u16;

constexpr int NBATCH = 8, SQ = 240, DM = 640, NH = 32, SKV = 1024, DH = 128, HD = NH * DH;
constexpr int MROWS = NBATCH * SQ;
constexpr int NW = 8, QBLK = 32, KVBLK = 64;
constexpr float SCALE = 0.088388347648318440f;
constexpr int SHM_T = KVBLK * DH * 2;
constexpr int SHM_ATTN = 4 * SHM_T;

#define SBAR() __builtin_amdgcn_sched_barrier(0)
#define KSWZ(row, colB) ((row) * 256 + ((colB) ^ (((row) & 7) << 4)))

typedef float  f32x2_t  __attribute__((ext_vector_type(2)));
typedef __bf16 bf16x2_t __attribute__((ext_vector_type(2)));
__device__ __forceinline__ unsigned cvtpk(float lo, float hi) {
  f32x2_t v = {lo, hi}; bf16x2_t b = __builtin_convertvector(v, bf16x2_t); return __builtin_bit_cast(unsigned, b);
}
__device__ __forceinline__ bf16x8 ld8(const u16* p) { return *reinterpret_cast<const bf16x8*>(p); }

__device__ __forceinline__ int v_st(int k, int c) {
  const int kk = (k & ~0xC) | ((k & 4) << 1) | ((k & 8) >> 1);
  return ((kk >> 3) * 4 + (c >> 5)) * 512 + ((kk & 7) * 32 + (c & 31)) * 2;
}
__device__ __forceinline__ int v_rd_base(int lane) {
  return ((lane & 3) << 3) | (((lane >> 2) & 3) << 6) | (((lane >> 4) & 1) << 5) | (((lane >> 5) & 1) << 8);
}
constexpr int v_rd_off(int d0, int ks, int half) { return d0 * 512 + ks * 4096 + half * 2048; }
template <int OFF> __device__ __forceinline__ s16x4 tr_read(int vb) {
  s16x4 r; asm volatile("ds_read_b64_tr_b16 %0, %1 offset:%2" : "=&v"(r) : "v"(vb), "i"(OFF) : "memory"); return r;
}
#define PKLH(L, H) (bf16x8){L[0], L[1], L[2], L[3], H[0], H[1], H[2], H[3]}

template <int D0>
__device__ __forceinline__ void tmm_one(f32x16& od, int vb, bf16x8 b0, bf16x8 b1, bf16x8 b2, bf16x8 b3) {
  const s16x4 l0 = tr_read<v_rd_off(D0, 0, 0)>(vb), h0 = tr_read<v_rd_off(D0, 0, 1)>(vb);
  const s16x4 l1 = tr_read<v_rd_off(D0, 1, 0)>(vb), h1 = tr_read<v_rd_off(D0, 1, 1)>(vb);
  const s16x4 l2 = tr_read<v_rd_off(D0, 2, 0)>(vb), h2 = tr_read<v_rd_off(D0, 2, 1)>(vb);
  const s16x4 l3 = tr_read<v_rd_off(D0, 3, 0)>(vb), h3 = tr_read<v_rd_off(D0, 3, 1)>(vb);
  asm volatile("s_waitcnt lgkmcnt(0)" ::: "memory"); SBAR();
  od = __builtin_amdgcn_mfma_f32_32x32x16_bf16(PKLH(l0, h0), b0, od, 0, 0, 0);
  od = __builtin_amdgcn_mfma_f32_32x32x16_bf16(PKLH(l1, h1), b1, od, 0, 0, 0);
  od = __builtin_amdgcn_mfma_f32_32x32x16_bf16(PKLH(l2, h2), b2, od, 0, 0, 0);
  od = __builtin_amdgcn_mfma_f32_32x32x16_bf16(PKLH(l3, h3), b3, od, 0, 0, 0);
}
__device__ __forceinline__ void tmm(f32x16* o, int vb, bf16x8 b0, bf16x8 b1, bf16x8 b2, bf16x8 b3) {
  tmm_one<0>(o[0], vb, b0, b1, b2, b3); tmm_one<1>(o[1], vb, b0, b1, b2, b3);
  tmm_one<2>(o[2], vb, b0, b1, b2, b3); tmm_one<3>(o[3], vb, b0, b1, b2, b3);
}

#define PK4(P, BASE, OUT) do { unsigned a0 = cvtpk(P[BASE + 0], P[BASE + 1]), a1 = cvtpk(P[BASE + 2], P[BASE + 3]);   \
    unsigned b0 = cvtpk(P[BASE + 4], P[BASE + 5]), b1 = cvtpk(P[BASE + 6], P[BASE + 7]);                              \
    auto r0 = __builtin_amdgcn_permlane32_swap(a0, b0, false, false); auto r1 = __builtin_amdgcn_permlane32_swap(a1, b1, false, false); \
    u32x4 w = {r0[0], r1[0], r0[1], r1[1]}; OUT = *reinterpret_cast<bf16x8*>(&w); } while (0)

__device__ __forceinline__ void partialSM(f32x16& p0, f32x16& p1, float& m_reg, float& mn, float& alpha) {
  constexpr float C = SCALE * 1.4426950408889634f;
  float pmax = p0[0];
#pragma unroll
  for (int r = 1; r < 16; ++r) pmax = fmaxf(pmax, p0[r]);
#pragma unroll
  for (int r = 0; r < 16; ++r) pmax = fmaxf(pmax, p1[r]);
  { auto rr = __builtin_amdgcn_permlane32_swap(__float_as_uint(pmax), __float_as_uint(pmax), false, false);
    pmax = fmaxf(__uint_as_float(rr[0]), __uint_as_float(rr[1])); }
  if (__all(pmax <= m_reg)) { mn = m_reg; alpha = 1.f; }
  else { mn = fmaxf(m_reg, pmax); alpha = __builtin_amdgcn_exp2f((m_reg - mn) * C); m_reg = mn; }
  const float mnC = -mn * C;
#pragma unroll
  for (int r = 0; r < 16; ++r) p0[r] = fmaf(p0[r], C, mnC);
#pragma unroll
  for (int r = 0; r < 16; ++r) p1[r] = fmaf(p1[r], C, mnC);
#pragma unroll
  for (int r = 0; r < 16; ++r) p0[r] = __builtin_amdgcn_exp2f(p0[r]);
}
__device__ __forceinline__ void finishSM(f32x16& p0, f32x16& p1, float alpha, float& l_reg,
                                         bf16x8& pa0, bf16x8& pa1, bf16x8& pa2, bf16x8& pa3) {
#pragma unroll
  for (int r = 0; r < 16; ++r) p1[r] = __builtin_amdgcn_exp2f(p1[r]);
  float ps = 0;
#pragma unroll
  for (int r = 0; r < 16; ++r) ps += p0[r];
#pragma unroll
  for (int r = 0; r < 16; ++r) ps += p1[r];
  { auto rr = __builtin_amdgcn_permlane32_swap(__float_as_uint(ps), __float_as_uint(ps), false, false);
    ps = __uint_as_float(rr[0]) + __uint_as_float(rr[1]); }
  l_reg = l_reg * alpha + ps;
  PK4(p0, 0, pa0); PK4(p0, 8, pa1); PK4(p1, 0, pa2); PK4(p1, 8, pa3);
}
__device__ __forceinline__ void qkt(f32x16& p0, f32x16& p1, const char* Ks, const bf16x8* qr, int r32, int hi) {
  p0 = f32x16{}; p1 = f32x16{};
#pragma unroll
  for (int d0 = 0; d0 < 8; ++d0) {
    const int cb = (d0 * 16 + hi * 8) * 2;
    bf16x8 b0 = *reinterpret_cast<const bf16x8*>(Ks + KSWZ(r32, cb));
    bf16x8 b1 = *reinterpret_cast<const bf16x8*>(Ks + KSWZ(32 + r32, cb));
    p0 = __builtin_amdgcn_mfma_f32_32x32x16_bf16(b0, qr[d0], p0, 0, 0, 0);
    p1 = __builtin_amdgcn_mfma_f32_32x32x16_bf16(b1, qr[d0], p1, 0, 0, 0);
  }
}

__device__ __forceinline__ void store_row128(const f32x16* o, float sc, u16* rowp, bool ok, int hi) {
#pragma unroll
  for (int d0 = 0; d0 < 4; ++d0) {
#pragma unroll
    for (int m = 0; m < 2; ++m) {
      unsigned ax = cvtpk(o[d0][8 * m + 0] * sc, o[d0][8 * m + 1] * sc), ay = cvtpk(o[d0][8 * m + 2] * sc, o[d0][8 * m + 3] * sc);
      unsigned bx = cvtpk(o[d0][8 * m + 4] * sc, o[d0][8 * m + 5] * sc), by = cvtpk(o[d0][8 * m + 6] * sc, o[d0][8 * m + 7] * sc);
      auto rx = __builtin_amdgcn_permlane32_swap(ax, bx, false, false);
      auto ry = __builtin_amdgcn_permlane32_swap(ay, by, false, false);
      u32x4 w = {rx[0], ry[0], rx[1], ry[1]};
      if (ok) *reinterpret_cast<u32x4*>(rowp + d0 * 32 + m * 16 + hi * 8) = w;
    }
  }
}

__global__ __launch_bounds__(64 * NW, 1)
void attn_fused(const u16* __restrict__ xA, const u16* __restrict__ kA, const u16* __restrict__ vA, const u16* __restrict__ wqA,
                const u16* __restrict__ xB, const u16* __restrict__ kB, const u16* __restrict__ vB, const u16* __restrict__ wqB,
                u16* __restrict__ ows) {
  extern __shared__ __attribute__((aligned(16))) char lds[];
  const int bid = blockIdx.x;
  const int blk = bid >> 8, b = (bid >> 5) & 7, h = bid & 31;
  const u16* x  = blk ? xB : xA;
  const u16* Kh = (blk ? kB : kA) + (size_t)(b * NH + h) * SKV * DH;
  const u16* Vh = (blk ? vB : vA) + (size_t)(b * NH + h) * SKV * DH;
  const u16* wq = (blk ? wqB : wqA) + h * DH;
  u16* orow0 = ows + ((size_t)blk * MROWS + (size_t)b * SQ) * HD + h * DH;

  const int tid = threadIdx.x, wid = tid >> 6, lane = tid & 63, r32 = lane & 31, hi = lane >> 5;
  char* V_lds = lds; char* K_lds = lds + 2 * SHM_T;
  const int qrow = wid * QBLK + r32;
  const int qrow_c = qrow < SQ ? qrow : SQ - 1;
  const int sr = tid >> 4, sc = (tid & 15) * 8;
  const int vst0 = v_st(sr, sc), vst1 = v_st(32 + sr, sc);
  const int vb0 = (int)(uintptr_t)V_lds + v_rd_base(lane);

  struct { bf16x8 vs0, vs1, ks0, ks1; } sr_[2];
#define SLOAD(i, k0) do { sr_[i].vs0 = ld8(&Vh[(size_t)((k0) + sr) * DH + sc]); sr_[i].vs1 = ld8(&Vh[(size_t)((k0) + 32 + sr) * DH + sc]); \
    sr_[i].ks0 = ld8(&Kh[(size_t)((k0) + sr) * DH + sc]); sr_[i].ks1 = ld8(&Kh[(size_t)((k0) + 32 + sr) * DH + sc]); } while (0)
#define SWRITE(bb, i) do { *(bf16x8*)(V_lds + (bb) * SHM_T + vst0) = sr_[i].vs0; *(bf16x8*)(V_lds + (bb) * SHM_T + vst1) = sr_[i].vs1; \
    const int kc = sc * 2; *(bf16x8*)(K_lds + (bb) * SHM_T + KSWZ(sr, kc)) = sr_[i].ks0;                                              \
    *(bf16x8*)(K_lds + (bb) * SHM_T + KSWZ(32 + sr, kc)) = sr_[i].ks1; } while (0)

  bf16x8 qr[8];
  {
    const u16* xr = x + ((size_t)b * SQ + qrow_c) * DM + hi * 8;
    f32x16 aq[4] = {};
    bf16x8 w0 = ld8(&wq[(size_t)sr * HD + sc]), w1 = ld8(&wq[(size_t)(32 + sr) * HD + sc]);
    bf16x8 xf0[4], xf1[4];
#pragma unroll
    for (int ks = 0; ks < 4; ++ks) xf0[ks] = ld8(xr + ks * 16);
    *(bf16x8*)(V_lds + vst0) = w0; *(bf16x8*)(V_lds + vst1) = w1;
    SLOAD(0, 0); SLOAD(1, KVBLK);
    __syncthreads();
    constexpr int NC = DM / KVBLK;
#pragma unroll
    for (int c = 0; c < NC; c += 2) {
      w0 = ld8(&wq[(size_t)((c + 1) * KVBLK + sr) * HD + sc]); w1 = ld8(&wq[(size_t)((c + 1) * KVBLK + 32 + sr) * HD + sc]);
#pragma unroll
      for (int ks = 0; ks < 4; ++ks) xf1[ks] = ld8(xr + (c + 1) * KVBLK + ks * 16);
      tmm(aq, vb0, xf0[0], xf0[1], xf0[2], xf0[3]);
      *(bf16x8*)(V_lds + SHM_T + vst0) = w0; *(bf16x8*)(V_lds + SHM_T + vst1) = w1;
      __syncthreads();
      if (c + 2 < NC) {
        w0 = ld8(&wq[(size_t)((c + 2) * KVBLK + sr) * HD + sc]); w1 = ld8(&wq[(size_t)((c + 2) * KVBLK + 32 + sr) * HD + sc]);
#pragma unroll
        for (int ks = 0; ks < 4; ++ks) xf0[ks] = ld8(xr + (c + 2) * KVBLK + ks * 16);
      }
      tmm(aq, vb0 + SHM_T, xf1[0], xf1[1], xf1[2], xf1[3]);
      if (c + 2 < NC) { *(bf16x8*)(V_lds + vst0) = w0; *(bf16x8*)(V_lds + vst1) = w1; }
      __syncthreads();
    }
#pragma unroll
    for (int nb = 0; nb < 4; ++nb) { PK4(aq[nb], 0, qr[2 * nb]); PK4(aq[nb], 8, qr[2 * nb + 1]); }
  }

  float m_reg = -1e30f, l_reg = 0.f;
  f32x16 o[4] = {};
#define RESC(a) do { if (__any((a) < 1.f)) { _Pragma("unroll") for (int d = 0; d < 4; ++d) _Pragma("unroll") for (int r = 0; r < 16; ++r) o[d][r] *= (a); } } while (0)
  f32x16 pA0, pA1, pB0, pB1; float mnA, mnB, alA, alB; bf16x8 pa0, pa1, pa2, pa3;
  constexpr int NT = SKV / KVBLK;
  asm volatile("s_waitcnt vmcnt(0)" ::: "memory");
  SWRITE(0, 0); __syncthreads();
  qkt(pA0, pA1, K_lds, qr, r32, hi); partialSM(pA0, pA1, m_reg, mnA, alA);
  SLOAD(0, 2 * KVBLK);
  SWRITE(1, 1); __syncthreads();
#pragma unroll 1
  for (int j = 1; j + 1 < NT; j += 2) {
    SBAR(); qkt(pB0, pB1, K_lds + SHM_T, qr, r32, hi);
    finishSM(pA0, pA1, alA, l_reg, pa0, pa1, pa2, pa3); SBAR();
    SLOAD(1, (j + 2) * KVBLK); SBAR();
    tmm(o, vb0, pa0, pa1, pa2, pa3); partialSM(pB0, pB1, m_reg, mnB, alB);
    __syncthreads(); asm volatile("s_waitcnt vmcnt(4)" ::: "memory"); SWRITE(0, 0);
    RESC(alB); __syncthreads();
    SBAR(); qkt(pA0, pA1, K_lds, qr, r32, hi);
    finishSM(pB0, pB1, alB, l_reg, pa0, pa1, pa2, pa3); SBAR();
    if (j + 3 < NT) SLOAD(0, (j + 3) * KVBLK);
    SBAR();
    tmm(o, vb0 + SHM_T, pa0, pa1, pa2, pa3); partialSM(pA0, pA1, m_reg, mnA, alA);
    __syncthreads();
    if (j + 3 < NT) asm volatile("s_waitcnt vmcnt(4)" ::: "memory"); else asm volatile("s_waitcnt vmcnt(0)" ::: "memory");
    SWRITE(1, 1);
    RESC(alA); __syncthreads();
  }
  SBAR(); qkt(pB0, pB1, K_lds + SHM_T, qr, r32, hi);
  finishSM(pA0, pA1, alA, l_reg, pa0, pa1, pa2, pa3); SBAR();
  tmm(o, vb0, pa0, pa1, pa2, pa3); partialSM(pB0, pB1, m_reg, mnB, alB);
  RESC(alB);
  finishSM(pB0, pB1, alB, l_reg, pa0, pa1, pa2, pa3); SBAR();
  tmm(o, vb0 + SHM_T, pa0, pa1, pa2, pa3);
  const float rl = __builtin_amdgcn_rcpf(l_reg);
  store_row128(o, rl, orow0 + (size_t)qrow_c * HD, qrow < SQ, hi);
#undef RESC
}

constexpr int OP_BM = 128, OP_BN = 128;
__global__ __launch_bounds__(256, 2)
void oproj(const u16* __restrict__ ows, const u16* __restrict__ woA, const u16* __restrict__ woB, u16* __restrict__ out) {
  __shared__ __attribute__((aligned(16))) char lds[2 * SHM_T];
  const int bid = blockIdx.x;
  const int blk = bid / 75, rem = bid % 75, nt = rem % 5, mt = rem / 5;
  const u16* wo = (blk ? woB : woA) + nt * OP_BN;
  const int tid = threadIdx.x, wid = tid >> 6, lane = tid & 63, r32 = lane & 31, hi = lane >> 5;
  const int row = mt * OP_BM + wid * 32 + r32;
  const u16* ar = ows + ((size_t)blk * MROWS + row) * HD + hi * 8;
  const int sr = tid >> 4, sc = (tid & 15) * 8;
  const int vst0 = v_st(sr, sc), vst1 = v_st(16 + sr, sc), vst2 = v_st(32 + sr, sc), vst3 = v_st(48 + sr, sc);
  const int vb0 = (int)(uintptr_t)lds + v_rd_base(lane);
  f32x16 acc[4] = {};
  bf16x8 w[4], af0[4], af1[4];
#define WLOAD(k0) do { _Pragma("unroll") for (int i = 0; i < 4; ++i) w[i] = ld8(&wo[(size_t)((k0) + 16 * i + sr) * DM + sc]); } while (0)
#define WWRITE(bb) do { *(bf16x8*)(lds + (bb) * SHM_T + vst0) = w[0]; *(bf16x8*)(lds + (bb) * SHM_T + vst1) = w[1]; \
    *(bf16x8*)(lds + (bb) * SHM_T + vst2) = w[2]; *(bf16x8*)(lds + (bb) * SHM_T + vst3) = w[3]; } while (0)
  WLOAD(0);
#pragma unroll
  for (int ks = 0; ks < 4; ++ks) af0[ks] = ld8(ar + ks * 16);
  WWRITE(0);
  __syncthreads();
  constexpr int NC = HD / KVBLK;
#pragma unroll 1
  for (int c = 0; c < NC; c += 2) {
    WLOAD((c + 1) * KVBLK);
#pragma unroll
    for (int ks = 0; ks < 4; ++ks) af1[ks] = ld8(ar + (c + 1) * KVBLK + ks * 16);
    tmm(acc, vb0, af0[0], af0[1], af0[2], af0[3]);
    WWRITE(1);
    __syncthreads();
    if (c + 2 < NC) {
      WLOAD((c + 2) * KVBLK);
#pragma unroll
      for (int ks = 0; ks < 4; ++ks) af0[ks] = ld8(ar + (c + 2) * KVBLK + ks * 16);
    }
    tmm(acc, vb0 + SHM_T, af1[0], af1[1], af1[2], af1[3]);
    if (c + 2 < NC) WWRITE(0);
    __syncthreads();
  }
  store_row128(acc, 1.f, out + (size_t)row * (2 * DM) + blk * DM + nt * OP_BN, true, hi);
#undef WLOAD
#undef WWRITE
}

extern "C" void kernel_launch(void* const* d_in, const int* in_sizes, int n_in,
                              void* d_out, int out_size, void* d_ws, size_t ws_size,
                              hipStream_t stream) {
  const u16* xA  = (const u16*)d_in[0]; const u16* kA = (const u16*)d_in[1]; const u16* vA = (const u16*)d_in[2];
  const u16* wqA = (const u16*)d_in[3]; const u16* woA = (const u16*)d_in[4];
  const u16* xB  = (const u16*)d_in[5]; const u16* kB = (const u16*)d_in[6]; const u16* vB = (const u16*)d_in[7];
  const u16* wqB = (const u16*)d_in[8]; const u16* woB = (const u16*)d_in[9];
  u16* ows = (u16*)d_ws;
  (void)hipFuncSetAttribute((const void*)attn_fused, hipFuncAttributeMaxDynamicSharedMemorySize, SHM_ATTN);
  attn_fused<<<dim3(2 * NBATCH * NH), dim3(64 * NW), SHM_ATTN, stream>>>(xA, kA, vA, wqA, xB, kB, vB, wqB, ows);
  oproj<<<dim3(2 * 15 * 5), dim3(256), 0, stream>>>(ows, woA, woB, (u16*)d_out);
}
```

```cpp
#include <hip/hip_runtime.h>
#include <hip/hip_bf16.h>
#include <stdint.h>

typedef short    bf16x8 __attribute__((ext_vector_type(8)));
typedef short    s16x4  __attribute__((ext_vector_type(4)));
typedef float    f32x16 __attribute__((ext_vector_type(16)));
typedef unsigned u32x4  __attribute__((ext_vector_type(4)));
typedef unsigned u32x2  __attribute__((ext_vector_type(2)));
typedef unsigned short u16;

constexpr int NBATCH = 8, SQ = 240, DM = 640, NH = 32, SKV = 1024, DH = 128, HD = NH * DH;
constexpr int HDP = HD + 128;
constexpr int MROWS = NBATCH * SQ;
constexpr int NW = 8, QBLK = 32, KVBLK = 64;
constexpr float SCALE = 0.088388347648318440f;
constexpr int SHM_T = KVBLK * DH * 2;
constexpr int SHM_ATTN = 8 * SHM_T;

#define SBAR() __builtin_amdgcn_sched_barrier(0)
#define KSWZ(row, colB) ((row) * 256 + ((colB) ^ (((row) & 7) << 4)))

typedef float  f32x2_t  __attribute__((ext_vector_type(2)));
typedef __bf16 bf16x2_t __attribute__((ext_vector_type(2)));
__device__ __forceinline__ unsigned cvtpk(float lo, float hi) {
  f32x2_t v = {lo, hi}; bf16x2_t b = __builtin_convertvector(v, bf16x2_t); return __builtin_bit_cast(unsigned, b);
}
__device__ __forceinline__ bf16x8 ld8(const u16* p) { return *reinterpret_cast<const bf16x8*>(p); }

__device__ __forceinline__ int v_st(int k, int c) {
  const int kk = (k & ~0xC) | ((k & 4) << 1) | ((k & 8) >> 1);
  return ((kk >> 3) * 4 + (c >> 5)) * 512 + ((kk & 7) * 32 + (c & 31)) * 2;
}
__device__ __forceinline__ int v_rd_base(int lane) {
  return ((lane & 3) << 3) | (((lane >> 2) & 3) << 6) | (((lane >> 4) & 1) << 5) | (((lane >> 5) & 1) << 8);
}
constexpr int v_rd_off(int d0, int ks, int half) { return d0 * 512 + ks * 4096 + half * 2048; }
template <int OFF> __device__ __forceinline__ s16x4 tr_read(int vb) {
  s16x4 r; asm volatile("ds_read_b64_tr_b16 %0, %1 offset:%2" : "=&v"(r) : "v"(vb), "i"(OFF) : "memory"); return r;
}
#define PKLH(L, H) (bf16x8){L[0], L[1], L[2], L[3], H[0], H[1], H[2], H[3]}

template <int D0>
__device__ __forceinline__ void tmm_one(f32x16& od, int vb, bf16x8 b0, bf16x8 b1, bf16x8 b2, bf16x8 b3) {
  const s16x4 l0 = tr_read<v_rd_off(D0, 0, 0)>(vb), h0 = tr_read<v_rd_off(D0, 0, 1)>(vb);
  const s16x4 l1 = tr_read<v_rd_off(D0, 1, 0)>(vb), h1 = tr_read<v_rd_off(D0, 1, 1)>(vb);
  const s16x4 l2 = tr_read<v_rd_off(D0, 2, 0)>(vb), h2 = tr_read<v_rd_off(D0, 2, 1)>(vb);
  const s16x4 l3 = tr_read<v_rd_off(D0, 3, 0)>(vb), h3 = tr_read<v_rd_off(D0, 3, 1)>(vb);
  asm volatile("s_waitcnt lgkmcnt(0)" ::: "memory"); SBAR();
  od = __builtin_amdgcn_mfma_f32_32x32x16_bf16(PKLH(l0, h0), b0, od, 0, 0, 0);
  od = __builtin_amdgcn_mfma_f32_32x32x16_bf16(PKLH(l1, h1), b1, od, 0, 0, 0);
  od = __builtin_amdgcn_mfma_f32_32x32x16_bf16(PKLH(l2, h2), b2, od, 0, 0, 0);
  od = __builtin_amdgcn_mfma_f32_32x32x16_bf16(PKLH(l3, h3), b3, od, 0, 0, 0);
}
__device__ __forceinline__ void tmm(f32x16* o, int vb, bf16x8 b0, bf16x8 b1, bf16x8 b2, bf16x8 b3) {
  tmm_one<0>(o[0], vb, b0, b1, b2, b3); tmm_one<1>(o[1], vb, b0, b1, b2, b3);
  tmm_one<2>(o[2], vb, b0, b1, b2, b3); tmm_one<3>(o[3], vb, b0, b1, b2, b3);
}

#define PK4(P, BASE, OUT) do { unsigned a0 = cvtpk(P[BASE + 0], P[BASE + 1]), a1 = cvtpk(P[BASE + 2], P[BASE + 3]);   \
    unsigned b0 = cvtpk(P[BASE + 4], P[BASE + 5]), b1 = cvtpk(P[BASE + 6], P[BASE + 7]);                              \
    auto r0 = __builtin_amdgcn_permlane32_swap(a0, b0, false, false); auto r1 = __builtin_amdgcn_permlane32_swap(a1, b1, false, false); \
    u32x4 w = {r0[0], r1[0], r0[1], r1[1]}; OUT = *reinterpret_cast<bf16x8*>(&w); } while (0)

__device__ __forceinline__ void partialSM(f32x16& p0, f32x16& p1, float& m_reg, float& mn, float& alpha) {
  constexpr float C = SCALE * 1.4426950408889634f;
  float pmax = p0[0];
#pragma unroll
  for (int r = 1; r < 16; ++r) pmax = fmaxf(pmax, p0[r]);
#pragma unroll
  for (int r = 0; r < 16; ++r) pmax = fmaxf(pmax, p1[r]);
  { auto rr = __builtin_amdgcn_permlane32_swap(__float_as_uint(pmax), __float_as_uint(pmax), false, false);
    pmax = fmaxf(__uint_as_float(rr[0]), __uint_as_float(rr[1])); }
  if (__all(pmax <= m_reg)) { mn = m_reg; alpha = 1.f; }
  else { mn = fmaxf(m_reg, pmax); alpha = __builtin_amdgcn_exp2f((m_reg - mn) * C); m_reg = mn; }
  const float mnC = -mn * C;
#pragma unroll
  for (int r = 0; r < 16; ++r) p0[r] = fmaf(p0[r], C, mnC);
#pragma unroll
  for (int r = 0; r < 16; ++r) p1[r] = fmaf(p1[r], C, mnC);
#pragma unroll
  for (int r = 0; r < 16; ++r) p0[r] = __builtin_amdgcn_exp2f(p0[r]);
}
__device__ __forceinline__ void finishSM(f32x16& p0, f32x16& p1, float alpha, float& l_reg,
                                         bf16x8& pa0, bf16x8& pa1, bf16x8& pa2, bf16x8& pa3) {
#pragma unroll
  for (int r = 0; r < 16; ++r) p1[r] = __builtin_amdgcn_exp2f(p1[r]);
  float ps = 0;
#pragma unroll
  for (int r = 0; r < 16; ++r) ps += p0[r];
#pragma unroll
  for (int r = 0; r < 16; ++r) ps += p1[r];
  { auto rr = __builtin_amdgcn_permlane32_swap(__float_as_uint(ps), __float_as_uint(ps), false, false);
    ps = __uint_as_float(rr[0]) + __uint_as_float(rr[1]); }
  l_reg = l_reg * alpha + ps;
  PK4(p0, 0, pa0); PK4(p0, 8, pa1); PK4(p1, 0, pa2); PK4(p1, 8, pa3);
}
__device__ __forceinline__ void qkt(f32x16& p0, f32x16& p1, const char* Ks, const bf16x8* qr, int r32, int hi) {
  p0 = f32x16{}; p1 = f32x16{};
#pragma unroll
  for (int d0 = 0; d0 < 8; ++d0) {
    const int cb = (d0 * 16 + hi * 8) * 2;
    bf16x8 b0 = *reinterpret_cast<const bf16x8*>(Ks + KSWZ(r32, cb));
    bf16x8 b1 = *reinterpret_cast<const bf16x8*>(Ks + KSWZ(32 + r32, cb));
    p0 = __builtin_amdgcn_mfma_f32_32x32x16_bf16(b0, qr[d0], p0, 0, 0, 0);
    p1 = __builtin_amdgcn_mfma_f32_32x32x16_bf16(b1, qr[d0], p1, 0, 0, 0);
  }
}

template <int D0LO, int D0HI>
__device__ __forceinline__ void qkt_part(f32x16& p0, f32x16& p1, const char* Ks, const bf16x8* qr, int r32, int hi) {
#pragma unroll
  for (int d0 = D0LO; d0 < D0HI; ++d0) {
    const int cb = (d0 * 16 + hi * 8) * 2;
    bf16x8 b0 = *reinterpret_cast<const bf16x8*>(Ks + KSWZ(r32, cb));
    bf16x8 b1 = *reinterpret_cast<const bf16x8*>(Ks + KSWZ(32 + r32, cb));
    p0 = __builtin_amdgcn_mfma_f32_32x32x16_bf16(b0, qr[d0], p0, 0, 0, 0);
    p1 = __builtin_amdgcn_mfma_f32_32x32x16_bf16(b1, qr[d0], p1, 0, 0, 0);
  }
}

__device__ __forceinline__ float max3f(float a, float b, float c) { float r; asm("v_max3_f32 %0, %1, %2, %3" : "=v"(r) : "v"(a), "v"(b), "v"(c)); return r; }
__device__ __forceinline__ float rowmax32(const f32x16& s0, const f32x16& s1) {
  float m0 = max3f(s0[0], s0[1], s1[0]), m1 = max3f(s0[2], s0[3], s1[1]);
#pragma unroll
  for (int r = 4; r < 16; r += 4) { m0 = max3f(m0, s0[r], s0[r + 1]); m1 = max3f(m1, s0[r + 2], s0[r + 3]); }
#pragma unroll
  for (int r = 2; r < 16; r += 4) { m0 = max3f(m0, s1[r], s1[r + 1]); if (r + 2 < 16) m1 = max3f(m1, s1[r + 2], s1[r + 3]); }
  m0 = fmaxf(m0, m1);
  auto rr = __builtin_amdgcn_permlane32_swap(__float_as_uint(m0), __float_as_uint(m0), false, false);
  return fmaxf(__uint_as_float(rr[0]), __uint_as_float(rr[1]));
}
constexpr float RTHR = 8.f;

#define KFRAG(Ks, d0, rb) (*reinterpret_cast<const bf16x8*>((Ks) + KSWZ((rb) * 32 + r32, ((d0) * 16 + hi * 8) * 2)))
template <bool FIN>
__device__ __forceinline__ void phaseA(f32x16& s0, f32x16& s1, const char* Ks, const bf16x8* qr, int r32, int hi,
                                       f32x16& p0, f32x16& p1, float alpha, float& l_reg,
                                       bf16x8& pa0, bf16x8& pa1, bf16x8& pa2, bf16x8& pa3) {
  bf16x8 ka[2], kb[2], kc[2];
  ka[0] = KFRAG(Ks, 0, 0); ka[1] = KFRAG(Ks, 0, 1);
  kb[0] = KFRAG(Ks, 1, 0); kb[1] = KFRAG(Ks, 1, 1);
  s0 = f32x16{}; s1 = f32x16{};
  float ps0 = 0.f, ps1 = 0.f;
  SBAR();
#define QSTEP(D0, CUR, NXT) do { if ((D0) + 2 < 8) { NXT[0] = KFRAG(Ks, (D0) + 2, 0); NXT[1] = KFRAG(Ks, (D0) + 2, 1); } SBAR(); \
    s0 = __builtin_amdgcn_mfma_f32_32x32x16_bf16(CUR[0], qr[D0], s0, 0, 0, 0);                                                  \
    s1 = __builtin_amdgcn_mfma_f32_32x32x16_bf16(CUR[1], qr[D0], s1, 0, 0, 0); } while (0)
  QSTEP(0, ka, kc);
  if (FIN) { _Pragma("unroll") for (int r = 0; r < 8; ++r) p1[r] = __builtin_amdgcn_exp2f(p1[r]); }
  SBAR();
  QSTEP(1, kb, ka);
  if (FIN) { _Pragma("unroll") for (int r = 8; r < 16; ++r) p1[r] = __builtin_amdgcn_exp2f(p1[r]); }
  SBAR();
  QSTEP(2, kc, kb);
  if (FIN) { _Pragma("unroll") for (int r = 0; r < 16; ++r) ps0 += p0[r]; PK4(p0, 0, pa0); }
  SBAR();
  QSTEP(3, ka, kc);
  if (FIN) { PK4(p0, 8, pa1); _Pragma("unroll") for (int r = 0; r < 8; ++r) ps1 += p1[r]; }
  SBAR();
  QSTEP(4, kb, ka);
  if (FIN) { _Pragma("unroll") for (int r = 8; r < 16; ++r) ps1 += p1[r]; PK4(p1, 0, pa2); }
  SBAR();
  QSTEP(5, kc, kb);
  if (FIN) { PK4(p1, 8, pa3); }
  SBAR();
  QSTEP(6, ka, kc);
  if (FIN) { float ps = ps0 + ps1; auto rr = __builtin_amdgcn_permlane32_swap(__float_as_uint(ps), __float_as_uint(ps), false, false);
             l_reg = l_reg * alpha + (__uint_as_float(rr[0]) + __uint_as_float(rr[1])); }
  SBAR();
  QSTEP(7, kb, ka);
#undef QSTEP
}

template <int D0> __device__ __forceinline__ void vread(s16x4* f, int vb) {
  f[0] = tr_read<v_rd_off(D0, 0, 0)>(vb); f[1] = tr_read<v_rd_off(D0, 0, 1)>(vb);
  f[2] = tr_read<v_rd_off(D0, 1, 0)>(vb); f[3] = tr_read<v_rd_off(D0, 1, 1)>(vb);
  f[4] = tr_read<v_rd_off(D0, 2, 0)>(vb); f[5] = tr_read<v_rd_off(D0, 2, 1)>(vb);
  f[6] = tr_read<v_rd_off(D0, 3, 0)>(vb); f[7] = tr_read<v_rd_off(D0, 3, 1)>(vb);
}
#define VMMA(OD, F) do { OD = __builtin_amdgcn_mfma_f32_32x32x16_bf16(PKLH(F[0], F[1]), b0, OD, 0, 0, 0); \
    OD = __builtin_amdgcn_mfma_f32_32x32x16_bf16(PKLH(F[2], F[3]), b1, OD, 0, 0, 0);                       \
    OD = __builtin_amdgcn_mfma_f32_32x32x16_bf16(PKLH(F[4], F[5]), b2, OD, 0, 0, 0);                       \
    OD = __builtin_amdgcn_mfma_f32_32x32x16_bf16(PKLH(F[6], F[7]), b3, OD, 0, 0, 0); } while (0)
template <bool START>
__device__ __forceinline__ void phaseB(f32x16* o, int vb, bf16x8 b0, bf16x8 b1, bf16x8 b2, bf16x8 b3,
                                       f32x16& s0, f32x16& s1, float& m_reg, float& alpha) {
  constexpr float C = SCALE * 1.4426950408889634f;
  s16x4 fa[8], fb[8];
  vread<0>(fa, vb);
  vread<1>(fb, vb);
  float pmax = 0.f;
  if (START) pmax = rowmax32(s0, s1);
  asm volatile("s_waitcnt lgkmcnt(8)" ::: "memory"); SBAR();
  VMMA(o[0], fa);
  float mnC = 0.f;
  if (START) {
    alpha = 1.f;
    if (__any((pmax - m_reg) * C > RTHR)) {
      const float mn = fmaxf(m_reg, pmax);
      alpha = __builtin_amdgcn_exp2f((m_reg - mn) * C); m_reg = mn;
    }
    mnC = -m_reg * C;
#pragma unroll
    for (int r = 0; r < 16; ++r) s0[r] = fmaf(s0[r], C, mnC);
  }
  SBAR();
  vread<2>(fa, vb);
  asm volatile("s_waitcnt lgkmcnt(8)" ::: "memory"); SBAR();
  VMMA(o[1], fb);
  if (START) {
#pragma unroll
    for (int r = 0; r < 16; ++r) s1[r] = fmaf(s1[r], C, mnC);
#pragma unroll
    for (int r = 0; r < 8; ++r) s0[r] = __builtin_amdgcn_exp2f(s0[r]);
  }
  SBAR();
  vread<3>(fb, vb);
  asm volatile("s_waitcnt lgkmcnt(8)" ::: "memory"); SBAR();
  VMMA(o[2], fa);
  if (START) {
#pragma unroll
    for (int r = 8; r < 16; ++r) s0[r] = __builtin_amdgcn_exp2f(s0[r]);
  }
  SBAR();
  asm volatile("s_waitcnt lgkmcnt(0)" ::: "memory"); SBAR();
  VMMA(o[3], fb);
}
#undef VMMA

__device__ __forceinline__ void store_row128(const f32x16* o, float sc, u16* rowp, bool ok, int hi) {
#pragma unroll
  for (int d0 = 0; d0 < 4; ++d0) {
#pragma unroll
    for (int m = 0; m < 2; ++m) {
      unsigned ax = cvtpk(o[d0][8 * m + 0] * sc, o[d0][8 * m + 1] * sc), ay = cvtpk(o[d0][8 * m + 2] * sc, o[d0][8 * m + 3] * sc);
      unsigned bx = cvtpk(o[d0][8 * m + 4] * sc, o[d0][8 * m + 5] * sc), by = cvtpk(o[d0][8 * m + 6] * sc, o[d0][8 * m + 7] * sc);
      auto rx = __builtin_amdgcn_permlane32_swap(ax, bx, false, false);
      auto ry = __builtin_amdgcn_permlane32_swap(ay, by, false, false);
      u32x4 w = {rx[0], ry[0], rx[1], ry[1]};
      if (ok) *reinterpret_cast<u32x4*>(rowp + d0 * 32 + m * 16 + hi * 8) = w;
    }
  }
}

__device__ __forceinline__ void dma16(const void* gsrc, int lds_dst) {
  unsigned keep;
  asm volatile("s_mov_b32 %0, m0\n\ts_mov_b32 m0, %2\n\ts_nop 0\n\tglobal_load_lds_dwordx4 %1, off\n\ts_mov_b32 m0, %0"
               : "=&s"(keep) : "v"(gsrc), "s"(lds_dst) : "memory");
}
template <int N> __device__ __forceinline__ void waitvm() { asm volatile("s_waitcnt vmcnt(%0)" :: "i"(N) : "memory"); }
#define BARRIER() __builtin_amdgcn_s_barrier()
#define TS(i) do { } while (0)

__global__ __launch_bounds__(64 * NW, 1)
void attn_fused(const u16* __restrict__ xA, const u16* __restrict__ kA, const u16* __restrict__ vA, const u16* __restrict__ wqA,
                const u16* __restrict__ xB, const u16* __restrict__ kB, const u16* __restrict__ vB, const u16* __restrict__ wqB,
                u16* __restrict__ ows) {
  extern __shared__ __attribute__((aligned(16))) char lds[];
  const int bid = blockIdx.x;
  const int blk = bid >> 8, b = (bid >> 5) & 7, h = bid & 31;
  const u16* x  = blk ? xB : xA;
  const u16* Kh = (blk ? kB : kA) + (size_t)(b * NH + h) * SKV * DH;
  const u16* Vh = (blk ? vB : vA) + (size_t)(b * NH + h) * SKV * DH;
  const u16* wq = (blk ? wqB : wqA) + h * DH;
  u16* orow0 = ows + ((size_t)blk * MROWS + (size_t)b * SQ) * HDP + h * DH;

  const int tid = threadIdx.x, wid = __builtin_amdgcn_readfirstlane(tid >> 6), lane = tid & 63, r32 = lane & 31, hi = lane >> 5;
  const int lds0 = (int)(uintptr_t)lds;
  const char* K_lds = lds;
  const int  V_base = lds0 + 4 * SHM_T;
  const int qrow = wid * QBLK + r32;
  const int qrow_c = qrow < SQ ? qrow : SQ - 1;
  const int vb0 = V_base + v_rd_base(lane);

  int k_src[2], v_srcR[2], v_col[2];
#pragma unroll
  for (int e = 0; e < 2; ++e) {
    const int p = 2 * wid + e;
    const int row = 4 * p + (lane >> 4), ch = (lane & 15) ^ (row & 7);
    k_src[e] = row * DH + ch * 8;
    const int sub = 2 * p + (lane >> 5), kk = (sub >> 2) * 8 + ((lane & 31) >> 2);
    v_srcR[e] = (kk & ~0xC) | ((kk & 4) << 1) | ((kk & 8) >> 1);
    v_col[e] = (sub & 3) * 32 + (lane & 3) * 8;
  }
#define DMA_K(j) do { _Pragma("unroll") for (int e = 0; e < 2; ++e) dma16(Kh + (size_t)(j) * KVBLK * DH + k_src[e], lds0 + ((j) & 3) * SHM_T + (2 * wid + e) * 1024); } while (0)
#define DMA_V(j) do { _Pragma("unroll") for (int e = 0; e < 2; ++e) dma16(Vh + (size_t)((j) * KVBLK + v_srcR[e]) * DH + v_col[e], V_base + ((10 + (j)) & 3) * SHM_T + (2 * wid + e) * 1024); } while (0)
#define DMA_W(c) do { _Pragma("unroll") for (int e = 0; e < 2; ++e) dma16(wq + (size_t)((c) * KVBLK + v_srcR[e]) * HD + v_col[e], V_base + ((c) & 3) * SHM_T + (2 * wid + e) * 1024); } while (0)

  bf16x8 qr[8];
  {
    const u16* xr = x + ((size_t)b * SQ + qrow_c) * DM + hi * 8;
    f32x16 aq[4] = {};
    bf16x8 xf[3][4];
#define XLOAD(c) do { _Pragma("unroll") for (int ks = 0; ks < 4; ++ks) xf[(c) % 3][ks] = ld8(xr + (c) * KVBLK + ks * 16); } while (0)
    XLOAD(0); XLOAD(1); SBAR();
    DMA_W(0); DMA_W(1); DMA_W(2);
    DMA_K(0); DMA_K(1); DMA_K(2);
    constexpr int NC = DM / KVBLK;
#pragma unroll
    for (int c = 0; c < NC; ++c) {
      TS(0);
      if (c == 0) waitvm<10>(); else if (c == 1) waitvm<14>(); else if (c == 2) waitvm<18>();
      else if (c <= 8) waitvm<16>(); else waitvm<12>();
      TS(7);
      BARRIER();
      TS(2);
      {
        const int vbq = V_base + v_rd_base(lane) + (c & 3) * SHM_T;
        tmm_one<0>(aq[0], vbq, xf[c % 3][0], xf[c % 3][1], xf[c % 3][2], xf[c % 3][3]); SBAR();
        if (c + 3 < NC) DMA_W(c + 3); else if (c + 3 < NC + 2) DMA_V(c + 3 - NC);
        SBAR();
        tmm_one<1>(aq[1], vbq, xf[c % 3][0], xf[c % 3][1], xf[c % 3][2], xf[c % 3][3]); SBAR();
        if (c + 2 < NC) XLOAD(c + 2);
        SBAR();
        tmm_one<2>(aq[2], vbq, xf[c % 3][0], xf[c % 3][1], xf[c % 3][2], xf[c % 3][3]);
        tmm_one<3>(aq[3], vbq, xf[c % 3][0], xf[c % 3][1], xf[c % 3][2], xf[c % 3][3]);
      }
    }
#undef XLOAD
#pragma unroll
    for (int nb = 0; nb < 4; ++nb) { PK4(aq[nb], 0, qr[2 * nb]); PK4(aq[nb], 8, qr[2 * nb + 1]); }
  }

  float m_reg = -1e30f, l_reg = 0.f;
  f32x16 o[4] = {};
#define RESC(a) do { if (__any((a) < 1.f)) { _Pragma("unroll") for (int d = 0; d < 4; ++d) _Pragma("unroll") for (int r = 0; r < 16; ++r) o[d][r] *= (a); } } while (0)
#define VSLOT(j) (((10 + (j)) & 3) * SHM_T)
  f32x16 pA0, pA1, pB0, pB1; float alA = 1.f, alB = 1.f; bf16x8 pa0, pa1, pa2, pa3;
  constexpr int NT = SKV / KVBLK;
  TS(0);
  waitvm<4>(); BARRIER();
  phaseA<false>(pA0, pA1, K_lds, qr, r32, hi, pB0, pB1, alB, l_reg, pa0, pa1, pa2, pa3);
  DMA_K(3); DMA_V(2); SBAR();
  { constexpr float C = SCALE * 1.4426950408889634f;
    m_reg = rowmax32(pA0, pA1);
    const float mnC = -m_reg * C;
#pragma unroll
    for (int r = 0; r < 16; ++r) { pA0[r] = fmaf(pA0[r], C, mnC); pA1[r] = fmaf(pA1[r], C, mnC); }
#pragma unroll
    for (int r = 0; r < 16; ++r) pA0[r] = __builtin_amdgcn_exp2f(pA0[r]);
    alA = 1.f; }
#pragma unroll 1
  for (int j = 1; j + 1 < NT; j += 2) {
    TS(5);
    if (j == 1) waitvm<6>(); else waitvm<8>();
    TS(1);
    BARRIER();
    TS(2);
    phaseA<true>(pB0, pB1, K_lds + ((j & 3) * SHM_T), qr, r32, hi, pA0, pA1, alA, l_reg, pa0, pa1, pa2, pa3);
    SBAR();
    if (j + 3 < NT) DMA_K(j + 3);
    DMA_V(j + 2); SBAR();
    TS(4);
    phaseB<true>(o, vb0 + VSLOT(j - 1), pa0, pa1, pa2, pa3, pB0, pB1, m_reg, alB);
    RESC(alB);
    TS(5);
    if (j + 1 == NT - 2) waitvm<6>(); else waitvm<8>();
    TS(1);
    BARRIER();
    TS(2);
    phaseA<true>(pA0, pA1, K_lds + (((j + 1) & 3) * SHM_T), qr, r32, hi, pB0, pB1, alB, l_reg, pa0, pa1, pa2, pa3);
    SBAR();
    if (j + 4 < NT) DMA_K(j + 4);
    if (j + 3 < NT) DMA_V(j + 3);
    SBAR();
    TS(4);
    phaseB<true>(o, vb0 + VSLOT(j), pa0, pa1, pa2, pa3, pA0, pA1, m_reg, alA);
    RESC(alA);
  }
  TS(5);
  waitvm<2>(); BARRIER(); SBAR();
  phaseA<true>(pB0, pB1, K_lds + (((NT - 1) & 3) * SHM_T), qr, r32, hi, pA0, pA1, alA, l_reg, pa0, pa1, pa2, pa3);
  SBAR();
  phaseB<true>(o, vb0 + VSLOT(NT - 2), pa0, pa1, pa2, pa3, pB0, pB1, m_reg, alB);
  RESC(alB);
  finishSM(pB0, pB1, alB, l_reg, pa0, pa1, pa2, pa3); SBAR();
  waitvm<0>(); BARRIER(); SBAR();
  { f32x16 d0_, d1_; float dm = 0.f, da = 1.f; phaseB<false>(o, vb0 + VSLOT(NT - 1), pa0, pa1, pa2, pa3, d0_, d1_, dm, da); }
  const float rl = __builtin_amdgcn_rcpf(l_reg);
  store_row128(o, rl, orow0 + (size_t)qrow_c * HDP, qrow < SQ, hi);
#undef RESC
#undef VSLOT
#undef DMA_K
#undef DMA_V
#undef DMA_W
}

typedef float f32x4 __attribute__((ext_vector_type(4)));
#define ABL_B true
constexpr int OP_BM = 128, OP_BN = 80, OP_BK = 64, OP_NST = 8;
constexpr int OP_BSZ = OP_BK * OP_BN * 2;
constexpr int OP_SHM = OP_NST * OP_BSZ;
__device__ __forceinline__ int opb_off(int k, int col) {
  return ((((k >> 3) * 5 + (col >> 4)) << 8) + ((k & 7) << 5) + ((col & 15) << 1)) ^ (((k >> 3) & 1) << 7);
}
template <int OFF> __device__ __forceinline__ s16x4 tr_read_o(int vb) {
  s16x4 r; asm volatile("ds_read_b64_tr_b16 %0, %1 offset:%2" : "=&v"(r) : "v"(vb), "i"(OFF) : "memory"); return r;
}
__device__ __forceinline__ void glds16(const void* g, char* l) {
  __builtin_amdgcn_global_load_lds((const unsigned*)g, (unsigned*)l, 16, 0, 0);
}
template <int N> __device__ __forceinline__ void wait_vm() { asm volatile("s_waitcnt vmcnt(%0)" :: "i"(N) : "memory"); }

__global__ __launch_bounds__(512, 2)
void oproj(const u16* __restrict__ ows, const u16* __restrict__ woA, const u16* __restrict__ woB, u16* __restrict__ out) {
  extern __shared__ __attribute__((aligned(16))) char lds[];
  const int L = (blockIdx.x & 7) * 30 + (blockIdx.x >> 3);
  const int u = L >> 3, nt = L & 7, blk = u / 15, mt = u % 15;
  const u16* wo = (blk ? woB : woA) + nt * OP_BN;
  const int tid = threadIdx.x, wid8 = __builtin_amdgcn_readfirstlane(tid >> 6), lane = tid & 63;
  const bool loader = wid8 >= 4;
  const int wid = wid8 & 3;
  const int g = lane >> 4, l16 = lane & 15;
  constexpr int NC = HD / OP_BK;
  const int koff = 0;
#define KROT(c) (((c) + koff) & 63)

  if (loader) {
    const int nbp = wid < 2 ? 3 : 2, bp0 = wid < 2 ? 3 * wid : 2 * wid + 2;
    int b_src[3];
#pragma unroll
    for (int i = 0; i < 3; ++i) {
      const int Lb = (bp0 + (i < nbp ? i : 0)) * 1024 + lane * 16;
      const int sidx = Lb >> 8, kg = sidx / 5, cb = sidx % 5;
      const int w = (Lb & 255) ^ ((kg & 1) << 7);
      const int k = kg * 8 + (w >> 5), col = cb * 16 + ((w & 31) >> 1);
      b_src[i] = k * DM + col;
    }
#define OP_ISSUE(c) do { char* st_ = lds + ((c) % OP_NST) * OP_BSZ; const u16* bg_ = wo + (size_t)KROT(c) * OP_BK * DM; \
    _Pragma("unroll") for (int i = 0; i < 3; ++i) if (i < nbp && ABL_B) glds16(bg_ + b_src[i], st_ + (bp0 + i) * 1024 + lane * 16); } while (0)
#pragma unroll
    for (int c = 0; c < OP_NST - 1; ++c) OP_ISSUE(c);
#pragma unroll 1
    for (int c = 0; c < NC - (OP_NST - 2); ++c) {
      if (wid < 2) wait_vm<3 * (OP_NST - 2)>(); else wait_vm<2 * (OP_NST - 2)>();
      __builtin_amdgcn_s_barrier();
      if (c + OP_NST - 1 < NC) OP_ISSUE(c + OP_NST - 1);
    }
#pragma unroll
    for (int j = OP_NST - 3; j >= 0; --j) {
      if (wid < 2) { switch (j) { case 5: wait_vm<15>(); break; case 4: wait_vm<12>(); break; case 3: wait_vm<9>(); break;
                                  case 2: wait_vm<6>(); break; case 1: wait_vm<3>(); break; default: wait_vm<0>(); } }
      else         { switch (j) { case 5: wait_vm<10>(); break; case 4: wait_vm<8>(); break; case 3: wait_vm<6>(); break;
                                  case 2: wait_vm<4>(); break; case 1: wait_vm<2>(); break; default: wait_vm<0>(); } }
      __builtin_amdgcn_s_barrier();
    }
#undef OP_ISSUE
    return;
  }

  const u16* ap0 = ows + ((size_t)blk * MROWS + mt * OP_BM + wid * 32 + l16) * HDP + g * 8;
  const u16* ap1 = ap0 + (size_t)16 * HDP;
  const int b_rd0 = opb_off(8 * g + (l16 >> 2), 4 * (l16 & 3));
  const int b_rd1 = opb_off(8 * g + 4 + (l16 >> 2), 4 * (l16 & 3));
  f32x4 acc[2][5] = {};
  bf16x8 af[4][4];
#define A_LOAD(slot, c) do { const int kk_ = KROT(c) * OP_BK; af[slot][0] = ld8(ap0 + kk_); af[slot][1] = ld8(ap0 + kk_ + 32); \
    af[slot][2] = ld8(ap1 + kk_); af[slot][3] = ld8(ap1 + kk_ + 32); } while (0)
#pragma unroll
  for (int i = 0; i < 4; ++i) { A_LOAD(i, i); SBAR(); }

#define OP_KSTEP(KOFF, A0, A1) do {                                                                   \
      const s16x4 l0 = tr_read_o<KOFF + 0 * 256>(bb0), h0 = tr_read_o<KOFF + 0 * 256>(bb1);           \
      const s16x4 l1 = tr_read_o<KOFF + 1 * 256>(bb0), h1 = tr_read_o<KOFF + 1 * 256>(bb1);           \
      const s16x4 l2 = tr_read_o<KOFF + 2 * 256>(bb0), h2 = tr_read_o<KOFF + 2 * 256>(bb1);           \
      const s16x4 l3 = tr_read_o<KOFF + 3 * 256>(bb0), h3 = tr_read_o<KOFF + 3 * 256>(bb1);           \
      const s16x4 l4 = tr_read_o<KOFF + 4 * 256>(bb0), h4 = tr_read_o<KOFF + 4 * 256>(bb1);           \
      asm volatile("s_waitcnt lgkmcnt(0)" ::: "memory"); SBAR();                                      \
      acc[0][0] = __builtin_amdgcn_mfma_f32_16x16x32_bf16(PKLH(l0, h0), A0, acc[0][0], 0, 0, 0);      \
      acc[1][0] = __builtin_amdgcn_mfma_f32_16x16x32_bf16(PKLH(l0, h0), A1, acc[1][0], 0, 0, 0);      \
      acc[0][1] = __builtin_amdgcn_mfma_f32_16x16x32_bf16(PKLH(l1, h1), A0, acc[0][1], 0, 0, 0);      \
      acc[1][1] = __builtin_amdgcn_mfma_f32_16x16x32_bf16(PKLH(l1, h1), A1, acc[1][1], 0, 0, 0);      \
      acc[0][2] = __builtin_amdgcn_mfma_f32_16x16x32_bf16(PKLH(l2, h2), A0, acc[0][2], 0, 0, 0);      \
      acc[1][2] = __builtin_amdgcn_mfma_f32_16x16x32_bf16(PKLH(l2, h2), A1, acc[1][2], 0, 0, 0);      \
      acc[0][3] = __builtin_amdgcn_mfma_f32_16x16x32_bf16(PKLH(l3, h3), A0, acc[0][3], 0, 0, 0);      \
      acc[1][3] = __builtin_amdgcn_mfma_f32_16x16x32_bf16(PKLH(l3, h3), A1, acc[1][3], 0, 0, 0);      \
      acc[0][4] = __builtin_amdgcn_mfma_f32_16x16x32_bf16(PKLH(l4, h4), A0, acc[0][4], 0, 0, 0);      \
      acc[1][4] = __builtin_amdgcn_mfma_f32_16x16x32_bf16(PKLH(l4, h4), A1, acc[1][4], 0, 0, 0); } while (0)

#pragma unroll 1
  for (int c0 = 0; c0 < NC - 4; c0 += 4) {
#pragma unroll
    for (int uu = 0; uu < 4; ++uu) {
      const int c = c0 + uu;
      __builtin_amdgcn_s_barrier();
      const char* st = lds + (c % OP_NST) * OP_BSZ;
      const int bb0 = (int)(uintptr_t)st + b_rd0, bb1 = (int)(uintptr_t)st + b_rd1;
      OP_KSTEP(0, af[uu][0], af[uu][2]);
      OP_KSTEP(5120, af[uu][1], af[uu][3]);
      A_LOAD(uu, c + 4); SBAR();
    }
  }
#pragma unroll
  for (int uu = 0; uu < 4; ++uu) {
    const int c = NC - 4 + uu;
    __builtin_amdgcn_s_barrier();
    const char* st = lds + (c % OP_NST) * OP_BSZ;
    const int bb0 = (int)(uintptr_t)st + b_rd0, bb1 = (int)(uintptr_t)st + b_rd1;
    OP_KSTEP(0, af[uu][0], af[uu][2]);
    OP_KSTEP(5120, af[uu][1], af[uu][3]);
  }
#undef OP_KSTEP
#undef A_LOAD
#pragma unroll
  for (int rb = 0; rb < 2; ++rb) {
    u16* op = out + (size_t)(mt * OP_BM + wid * 32 + rb * 16 + l16) * (2 * DM) + blk * DM + nt * OP_BN + 4 * g;
#pragma unroll
    for (int cb = 0; cb < 5; ++cb) {
      u32x2 w = {cvtpk(acc[rb][cb][0], acc[rb][cb][1]), cvtpk(acc[rb][cb][2], acc[rb][cb][3])};
      *reinterpret_cast<u32x2*>(op + cb * 16) = w;
    }
  }
}

extern "C" void kernel_launch(void* const* d_in, const int* in_sizes, int n_in,
                              void* d_out, int out_size, void* d_ws, size_t ws_size,
                              hipStream_t stream) {
  const u16* xA  = (const u16*)d_in[0]; const u16* kA = (const u16*)d_in[1]; const u16* vA = (const u16*)d_in[2];
  const u16* wqA = (const u16*)d_in[3]; const u16* woA = (const u16*)d_in[4];
  const u16* xB  = (const u16*)d_in[5]; const u16* kB = (const u16*)d_in[6]; const u16* vB = (const u16*)d_in[7];
  const u16* wqB = (const u16*)d_in[8]; const u16* woB = (const u16*)d_in[9];
  u16* ows = (u16*)d_ws;
  (void)hipFuncSetAttribute((const void*)attn_fused, hipFuncAttributeMaxDynamicSharedMemorySize, SHM_ATTN);
  attn_fused<<<dim3(2 * NBATCH * NH), dim3(64 * NW), SHM_ATTN, stream>>>(xA, kA, vA, wqA, xB, kB, vB, wqB, ows);
  (void)hipFuncSetAttribute((const void*)oproj, hipFuncAttributeMaxDynamicSharedMemorySize, OP_SHM);
  oproj<<<dim3(240), dim3(512), OP_SHM, stream>>>(ows, woA, woB, (u16*)d_out);
}
```

```cpp
#include <hip/hip_runtime.h>
#include <hip/hip_bf16.h>
#include <stdint.h>
#include <utility>

typedef short    bf16x8 __attribute__((ext_vector_type(8)));
typedef short    s16x4  __attribute__((ext_vector_type(4)));
typedef float    f32x16 __attribute__((ext_vector_type(16)));
typedef unsigned u32x4  __attribute__((ext_vector_type(4)));
typedef unsigned u32x2  __attribute__((ext_vector_type(2)));
typedef unsigned short u16;

constexpr int NBATCH = 8, SQ = 240, DM = 640, NH = 32, SKV = 1024, DH = 128, HD = NH * DH;
constexpr int MROWS = NBATCH * SQ;

#define SBAR() __builtin_amdgcn_sched_barrier(0)

typedef float  f32x2_t  __attribute__((ext_vector_type(2)));
typedef __bf16 bf16x2_t __attribute__((ext_vector_type(2)));
__device__ __forceinline__ unsigned cvtpk(float lo, float hi) {
  f32x2_t v = {lo, hi}; bf16x2_t b = __builtin_convertvector(v, bf16x2_t); return __builtin_bit_cast(unsigned, b);
}

__device__ __forceinline__ int v_rd_base(int lane) {
  return ((lane & 3) << 3) | (((lane >> 2) & 3) << 6) | (((lane >> 4) & 1) << 5) | (((lane >> 5) & 1) << 8);
}
template <int OFF> __device__ __forceinline__ s16x4 tr_read(int vb) {
  s16x4 r; asm volatile("ds_read_b64_tr_b16 %0, %1 offset:%2" : "=&v"(r) : "v"(vb), "i"(OFF) : "memory"); return r;
}
#define PKLH(L, H) (bf16x8){L[0], L[1], L[2], L[3], H[0], H[1], H[2], H[3]}

__device__ __forceinline__ void dma16(const void* gsrc, int lds_dst) {
  unsigned keep;
  asm volatile("s_mov_b32 %0, m0\n\ts_mov_b32 m0, %2\n\ts_nop 0\n\tglobal_load_lds_dwordx4 %1, off\n\ts_mov_b32 m0, %0"
               : "=&s"(keep) : "v"(gsrc), "s"(lds_dst) : "memory");
}
template <int N> __device__ __forceinline__ void waitvm() { asm volatile("s_waitcnt vmcnt(%0)" :: "i"(N) : "memory"); }
#define BARRIER() __builtin_amdgcn_s_barrier()

namespace pw {
typedef __bf16   bf16x8 __attribute__((ext_vector_type(8)));
typedef __bf16   bf16x4 __attribute__((ext_vector_type(4)));
typedef __bf16   bf16x2 __attribute__((ext_vector_type(2)));
typedef float    f32x2  __attribute__((ext_vector_type(2)));
template <int... Is> using iseq = std::integer_sequence<int, Is...>;
template <int N> using make_iseq = std::make_integer_sequence<int, N>;
constexpr unsigned KRS = 256u, KTILEB = 64u * KRS;
#define KRING 0x0000u
#define VRING 0x8000u
#define SRD_WORD3 0x00020000
#define DI __device__ __forceinline__
#define GAP() __builtin_amdgcn_sched_barrier(0)
#define W4_ACLOB_HI "a128","a129","a130","a131","a132","a133","a134","a135","a136","a137","a138","a139","a140","a141","a142","a143","a144","a145","a146","a147","a148","a149","a150","a151","a152","a153","a154","a155","a156","a157","a158","a159","a160","a161","a162","a163","a164","a165","a166","a167","a168","a169","a170","a171","a172","a173","a174","a175","a176","a177","a178","a179","a180","a181","a182","a183","a184","a185","a186","a187","a188","a189","a190","a191","a192","a193","a194","a195","a196","a197","a198","a199","a200","a201","a202","a203","a204","a205","a206","a207","a208","a209","a210","a211","a212","a213","a214","a215","a216","a217","a218","a219","a220","a221","a222","a223","a224","a225","a226","a227","a228","a229","a230","a231","a232","a233","a234","a235","a236","a237","a238","a239","a240","a241","a242","a243","a244","a245","a246","a247","a248","a249","a250","a251","a252","a253","a254","a255"
#define W4_ACLOB "a0","a1","a2","a3","a4","a5","a6","a7","a8","a9","a10","a11","a12","a13","a14","a15","a16","a17","a18","a19","a20","a21","a22","a23","a24","a25","a26","a27","a28","a29","a30","a31","a32","a33","a34","a35","a36","a37","a38","a39","a40","a41","a42","a43","a44","a45","a46","a47","a48","a49","a50","a51","a52","a53","a54","a55","a56","a57","a58","a59","a60","a61","a62","a63","a64","a65","a66","a67","a68","a69","a70","a71","a72","a73","a74","a75","a76","a77","a78","a79","a80","a81","a82","a83","a84","a85","a86","a87","a88","a89","a90","a91","a92","a93","a94","a95","a96","a97","a98","a99","a100","a101","a102","a103","a104","a105","a106","a107","a108","a109","a110","a111","a112","a113","a114","a115","a116","a117","a118","a119","a120","a121","a122","a123","a124","a125","a126","a127"
#define W4_READ_O(DB, QB, o) do { switch (2 * (DB) + (QB)) { \
    case 0: asm volatile("v_accvgpr_read_b32 %0, a0\n\t" "v_accvgpr_read_b32 %1, a1\n\t" "v_accvgpr_read_b32 %2, a2\n\t" "v_accvgpr_read_b32 %3, a3\n\t" "v_accvgpr_read_b32 %4, a4\n\t" "v_accvgpr_read_b32 %5, a5\n\t" "v_accvgpr_read_b32 %6, a6\n\t" "v_accvgpr_read_b32 %7, a7\n\t" "v_accvgpr_read_b32 %8, a8\n\t" "v_accvgpr_read_b32 %9, a9\n\t" "v_accvgpr_read_b32 %10, a10\n\t" "v_accvgpr_read_b32 %11, a11\n\t" "v_accvgpr_read_b32 %12, a12\n\t" "v_accvgpr_read_b32 %13, a13\n\t" "v_accvgpr_read_b32 %14, a14\n\t" "v_accvgpr_read_b32 %15, a15\n\t" : "=v"((o)[0]), "=v"((o)[1]), "=v"((o)[2]), "=v"((o)[3]), "=v"((o)[4]), "=v"((o)[5]), "=v"((o)[6]), "=v"((o)[7]), "=v"((o)[8]), "=v"((o)[9]), "=v"((o)[10]), "=v"((o)[11]), "=v"((o)[12]), "=v"((o)[13]), "=v"((o)[14]), "=v"((o)[15])); break; \
    case 1: asm volatile("v_accvgpr_read_b32 %0, a16\n\t" "v_accvgpr_read_b32 %1, a17\n\t" "v_accvgpr_read_b32 %2, a18\n\t" "v_accvgpr_read_b32 %3, a19\n\t" "v_accvgpr_read_b32 %4, a20\n\t" "v_accvgpr_read_b32 %5, a21\n\t" "v_accvgpr_read_b32 %6, a22\n\t" "v_accvgpr_read_b32 %7, a23\n\t" "v_accvgpr_read_b32 %8, a24\n\t" "v_accvgpr_read_b32 %9, a25\n\t" "v_accvgpr_read_b32 %10, a26\n\t" "v_accvgpr_read_b32 %11, a27\n\t" "v_accvgpr_read_b32 %12, a28\n\t" "v_accvgpr_read_b32 %13, a29\n\t" "v_accvgpr_read_b32 %14, a30\n\t" "v_accvgpr_read_b32 %15, a31\n\t" : "=v"((o)[0]), "=v"((o)[1]), "=v"((o)[2]), "=v"((o)[3]), "=v"((o)[4]), "=v"((o)[5]), "=v"((o)[6]), "=v"((o)[7]), "=v"((o)[8]), "=v"((o)[9]), "=v"((o)[10]), "=v"((o)[11]), "=v"((o)[12]), "=v"((o)[13]), "=v"((o)[14]), "=v"((o)[15])); break; \
    case 2: asm volatile("v_accvgpr_read_b32 %0, a32\n\t" "v_accvgpr_read_b32 %1, a33\n\t" "v_accvgpr_read_b32 %2, a34\n\t" "v_accvgpr_read_b32 %3, a35\n\t" "v_accvgpr_read_b32 %4, a36\n\t" "v_accvgpr_read_b32 %5, a37\n\t" "v_accvgpr_read_b32 %6, a38\n\t" "v_accvgpr_read_b32 %7, a39\n\t" "v_accvgpr_read_b32 %8, a40\n\t" "v_accvgpr_read_b32 %9, a41\n\t" "v_accvgpr_read_b32 %10, a42\n\t" "v_accvgpr_read_b32 %11, a43\n\t" "v_accvgpr_read_b32 %12, a44\n\t" "v_accvgpr_read_b32 %13, a45\n\t" "v_accvgpr_read_b32 %14, a46\n\t" "v_accvgpr_read_b32 %15, a47\n\t" : "=v"((o)[0]), "=v"((o)[1]), "=v"((o)[2]), "=v"((o)[3]), "=v"((o)[4]), "=v"((o)[5]), "=v"((o)[6]), "=v"((o)[7]), "=v"((o)[8]), "=v"((o)[9]), "=v"((o)[10]), "=v"((o)[11]), "=v"((o)[12]), "=v"((o)[13]), "=v"((o)[14]), "=v"((o)[15])); break; \
    case 3: asm volatile("v_accvgpr_read_b32 %0, a48\n\t" "v_accvgpr_read_b32 %1, a49\n\t" "v_accvgpr_read_b32 %2, a50\n\t" "v_accvgpr_read_b32 %3, a51\n\t" "v_accvgpr_read_b32 %4, a52\n\t" "v_accvgpr_read_b32 %5, a53\n\t" "v_accvgpr_read_b32 %6, a54\n\t" "v_accvgpr_read_b32 %7, a55\n\t" "v_accvgpr_read_b32 %8, a56\n\t" "v_accvgpr_read_b32 %9, a57\n\t" "v_accvgpr_read_b32 %10, a58\n\t" "v_accvgpr_read_b32 %11, a59\n\t" "v_accvgpr_read_b32 %12, a60\n\t" "v_accvgpr_read_b32 %13, a61\n\t" "v_accvgpr_read_b32 %14, a62\n\t" "v_accvgpr_read_b32 %15, a63\n\t" : "=v"((o)[0]), "=v"((o)[1]), "=v"((o)[2]), "=v"((o)[3]), "=v"((o)[4]), "=v"((o)[5]), "=v"((o)[6]), "=v"((o)[7]), "=v"((o)[8]), "=v"((o)[9]), "=v"((o)[10]), "=v"((o)[11]), "=v"((o)[12]), "=v"((o)[13]), "=v"((o)[14]), "=v"((o)[15])); break; \
    case 4: asm volatile("v_accvgpr_read_b32 %0, a64\n\t" "v_accvgpr_read_b32 %1, a65\n\t" "v_accvgpr_read_b32 %2, a66\n\t" "v_accvgpr_read_b32 %3, a67\n\t" "v_accvgpr_read_b32 %4, a68\n\t" "v_accvgpr_read_b32 %5, a69\n\t" "v_accvgpr_read_b32 %6, a70\n\t" "v_accvgpr_read_b32 %7, a71\n\t" "v_accvgpr_read_b32 %8, a72\n\t" "v_accvgpr_read_b32 %9, a73\n\t" "v_accvgpr_read_b32 %10, a74\n\t" "v_accvgpr_read_b32 %11, a75\n\t" "v_accvgpr_read_b32 %12, a76\n\t" "v_accvgpr_read_b32 %13, a77\n\t" "v_accvgpr_read_b32 %14, a78\n\t" "v_accvgpr_read_b32 %15, a79\n\t" : "=v"((o)[0]), "=v"((o)[1]), "=v"((o)[2]), "=v"((o)[3]), "=v"((o)[4]), "=v"((o)[5]), "=v"((o)[6]), "=v"((o)[7]), "=v"((o)[8]), "=v"((o)[9]), "=v"((o)[10]), "=v"((o)[11]), "=v"((o)[12]), "=v"((o)[13]), "=v"((o)[14]), "=v"((o)[15])); break; \
    case 5: asm volatile("v_accvgpr_read_b32 %0, a80\n\t" "v_accvgpr_read_b32 %1, a81\n\t" "v_accvgpr_read_b32 %2, a82\n\t" "v_accvgpr_read_b32 %3, a83\n\t" "v_accvgpr_read_b32 %4, a84\n\t" "v_accvgpr_read_b32 %5, a85\n\t" "v_accvgpr_read_b32 %6, a86\n\t" "v_accvgpr_read_b32 %7, a87\n\t" "v_accvgpr_read_b32 %8, a88\n\t" "v_accvgpr_read_b32 %9, a89\n\t" "v_accvgpr_read_b32 %10, a90\n\t" "v_accvgpr_read_b32 %11, a91\n\t" "v_accvgpr_read_b32 %12, a92\n\t" "v_accvgpr_read_b32 %13, a93\n\t" "v_accvgpr_read_b32 %14, a94\n\t" "v_accvgpr_read_b32 %15, a95\n\t" : "=v"((o)[0]), "=v"((o)[1]), "=v"((o)[2]), "=v"((o)[3]), "=v"((o)[4]), "=v"((o)[5]), "=v"((o)[6]), "=v"((o)[7]), "=v"((o)[8]), "=v"((o)[9]), "=v"((o)[10]), "=v"((o)[11]), "=v"((o)[12]), "=v"((o)[13]), "=v"((o)[14]), "=v"((o)[15])); break; \
    case 6: asm volatile("v_accvgpr_read_b32 %0, a96\n\t" "v_accvgpr_read_b32 %1, a97\n\t" "v_accvgpr_read_b32 %2, a98\n\t" "v_accvgpr_read_b32 %3, a99\n\t" "v_accvgpr_read_b32 %4, a100\n\t" "v_accvgpr_read_b32 %5, a101\n\t" "v_accvgpr_read_b32 %6, a102\n\t" "v_accvgpr_read_b32 %7, a103\n\t" "v_accvgpr_read_b32 %8, a104\n\t" "v_accvgpr_read_b32 %9, a105\n\t" "v_accvgpr_read_b32 %10, a106\n\t" "v_accvgpr_read_b32 %11, a107\n\t" "v_accvgpr_read_b32 %12, a108\n\t" "v_accvgpr_read_b32 %13, a109\n\t" "v_accvgpr_read_b32 %14, a110\n\t" "v_accvgpr_read_b32 %15, a111\n\t" : "=v"((o)[0]), "=v"((o)[1]), "=v"((o)[2]), "=v"((o)[3]), "=v"((o)[4]), "=v"((o)[5]), "=v"((o)[6]), "=v"((o)[7]), "=v"((o)[8]), "=v"((o)[9]), "=v"((o)[10]), "=v"((o)[11]), "=v"((o)[12]), "=v"((o)[13]), "=v"((o)[14]), "=v"((o)[15])); break; \
    case 7: asm volatile("v_accvgpr_read_b32 %0, a112\n\t" "v_accvgpr_read_b32 %1, a113\n\t" "v_accvgpr_read_b32 %2, a114\n\t" "v_accvgpr_read_b32 %3, a115\n\t" "v_accvgpr_read_b32 %4, a116\n\t" "v_accvgpr_read_b32 %5, a117\n\t" "v_accvgpr_read_b32 %6, a118\n\t" "v_accvgpr_read_b32 %7, a119\n\t" "v_accvgpr_read_b32 %8, a120\n\t" "v_accvgpr_read_b32 %9, a121\n\t" "v_accvgpr_read_b32 %10, a122\n\t" "v_accvgpr_read_b32 %11, a123\n\t" "v_accvgpr_read_b32 %12, a124\n\t" "v_accvgpr_read_b32 %13, a125\n\t" "v_accvgpr_read_b32 %14, a126\n\t" "v_accvgpr_read_b32 %15, a127\n\t" : "=v"((o)[0]), "=v"((o)[1]), "=v"((o)[2]), "=v"((o)[3]), "=v"((o)[4]), "=v"((o)[5]), "=v"((o)[6]), "=v"((o)[7]), "=v"((o)[8]), "=v"((o)[9]), "=v"((o)[10]), "=v"((o)[11]), "=v"((o)[12]), "=v"((o)[13]), "=v"((o)[14]), "=v"((o)[15])); break; \
    } } while (0)

DI unsigned rfl(unsigned x) { return __builtin_amdgcn_readfirstlane(x); }
DI unsigned cvtpk(float lo, float hi) {
  f32x2 v = {lo, hi}; bf16x2 b = __builtin_convertvector(v, bf16x2); return __builtin_bit_cast(unsigned, b);
}
DI float max3f(float a, float b, float c) { float r; asm("v_max3_f32 %0, %1, %2, %3" : "=v"(r) : "v"(a), "v"(b), "v"(c)); return r; }
DI float max2f(float a, float b) { float r; asm("v_max_f32_e32 %0, %1, %2" : "=v"(r) : "v"(a), "v"(b)); return r; }
DI float fadd_s(float a, float b) { float r; asm("v_add_f32_e32 %0, %1, %2" : "=v"(r) : "v"(a), "v"(b)); return r; }
DI float fmul_s(float a, float b) { float r; asm("v_mul_f32_e32 %0, %1, %2" : "=v"(r) : "v"(a), "v"(b)); return r; }
DI float fsub_s(float a, float b) { float r; asm("v_sub_f32_e32 %0, %1, %2" : "=v"(r) : "v"(a), "v"(b)); return r; }
DI float w4_aone() { unsigned l; asm volatile("v_mbcnt_lo_u32_b32 %0, -1, 0\n\tv_mbcnt_hi_u32_b32 %0, -1, %0" : "=v"(l)); return l < 32u ? 1.f : 0.f; }
DI void negm_bcast(f32x16& negm, float aone, float neg_mhat) {
  asm volatile("s_nop 1\n\tv_mfma_f32_32x32x2_f32 %0, %1, %2, 0" : "+v"(negm) : "v"(aone), "v"(neg_mhat));
}

struct W4C {
  unsigned vkb[4], vvb, vksrc[2], vvsrc, m0k, m0v, sofk, sofv, ldsb;
  __amdgpu_buffer_rsrc_t srdK, srdV;
  __amdgpu_buffer_rsrc_t srdKn, srdVn;
  int T; float THR;
  f32x16 S[2][2][2];
  f32x16 NEGM[2];
  bf16x8 Vf[4][4];
  bf16x8 Pk[2][4];
  bf16x4 vlo[4][4], vup[4][4];
  float mhat[2], lsum[2], fct[2]; bool pend;
  float rs0[2], rs1[2], pmx0[2], pmx1[2], rmax[2], delta[2];
  unsigned pm0[8], psf[8];
};

constexpr int AO_(int db, int qb) { return 16 * (2 * db + qb); }
constexpr int AQ_(int qb, int d0) { return 128 + 32 * qb + 4 * d0; }
constexpr int AK_(int kb, int d0) { return 192 + 32 * kb + 4 * d0; }

template <int I> DI void tr_read(W4C& c, unsigned vslot) {
  constexpr int ks = I >> 3, db = (I >> 1) & 3;
  if constexpr ((I & 1) == 0) asm volatile("ds_read_b64_tr_b16 %0, %1 offset:%2" : "=v"(c.vlo[db][ks]) : "v"(c.vvb + vslot), "i"(4096 * ks + 512 * db) : "memory");
  else                        asm volatile("ds_read_b64_tr_b16 %0, %1 offset:%2" : "=v"(c.vup[db][ks]) : "v"(c.vvb + vslot), "i"(4096 * ks + 2048 + 512 * db) : "memory");
}
template <int I> DI void k_read(W4C& c, unsigned slot) {
  constexpr int kb = I >> 3, d0 = I & 7;
  asm volatile("ds_read_b128 a[%c1:%c2], %0 offset:%c3" :: "v"(c.vkb[d0 & 3] + slot), "i"(AK_(kb, d0)), "i"(AK_(kb, d0) + 3), "i"(8192 * kb + 128 * (d0 >> 2)) : "memory", W4_ACLOB_HI);
}
template <int I> DI void q_write1(const u32x4& w) {
  constexpr int aq = AQ_(I >> 3, I & 7);
  asm volatile("v_accvgpr_write_b32 a[%c4], %0\n\tv_accvgpr_write_b32 a[%c5], %1\n\tv_accvgpr_write_b32 a[%c6], %2\n\tv_accvgpr_write_b32 a[%c7], %3"
               :: "v"(w[0]), "v"(w[1]), "v"(w[2]), "v"(w[3]), "i"(aq), "i"(aq + 1), "i"(aq + 2), "i"(aq + 3) : W4_ACLOB_HI);
}
DI void q_write(const int qb, const int d0, const u32x4& w) {
  switch (8 * qb + d0) {
    case 0: q_write1<0>(w); break;   case 1: q_write1<1>(w); break;   case 2: q_write1<2>(w); break;   case 3: q_write1<3>(w); break;
    case 4: q_write1<4>(w); break;   case 5: q_write1<5>(w); break;   case 6: q_write1<6>(w); break;   case 7: q_write1<7>(w); break;
    case 8: q_write1<8>(w); break;   case 9: q_write1<9>(w); break;   case 10: q_write1<10>(w); break; case 11: q_write1<11>(w); break;
    case 12: q_write1<12>(w); break; case 13: q_write1<13>(w); break; case 14: q_write1<14>(w); break; case 15: q_write1<15>(w); break;
  }
}
template <int X, bool FIRST, int M> DI void qk_mfma(W4C& c) {
  constexpr int d0 = M >> 2, kb = (M >> 1) & 1, qb = M & 1, ak = AK_(kb, d0), aq = AQ_(qb, d0);
  if constexpr (d0 == 0) {
    if constexpr (FIRST) asm volatile("v_mfma_f32_32x32x16_bf16 %0, a[%c1:%c2], a[%c3:%c4], 0" : "=&v"(c.S[X][kb][qb]) : "i"(ak), "i"(ak + 3), "i"(aq), "i"(aq + 3) : W4_ACLOB, W4_ACLOB_HI);
    else                 asm volatile("v_mfma_f32_32x32x16_bf16 %0, a[%c2:%c3], a[%c4:%c5], %1" : "=&v"(c.S[X][kb][qb]) : "v"(c.NEGM[qb]), "i"(ak), "i"(ak + 3), "i"(aq), "i"(aq + 3) : W4_ACLOB, W4_ACLOB_HI);
  } else asm volatile("v_mfma_f32_32x32x16_bf16 %0, a[%c1:%c2], a[%c3:%c4], %0" : "+v"(c.S[X][kb][qb]) : "i"(ak), "i"(ak + 3), "i"(aq), "i"(aq + 3) : W4_ACLOB, W4_ACLOB_HI);
}
template <int M, bool ZERO> DI void pv_mfma(W4C& c) {
  constexpr int ks = M >> 3, db = (M >> 1) & 3, qb = M & 1, ao = AO_(db, qb);
  if constexpr (ZERO && ks == 0) asm volatile("v_mfma_f32_32x32x16_bf16 a[%c2:%c3], %0, %1, 0" :: "v"(c.Vf[db][ks]), "v"(c.Pk[qb][ks]), "i"(ao), "i"(ao + 15) : W4_ACLOB);
  else                         asm volatile("v_mfma_f32_32x32x16_bf16 a[%c2:%c3], %0, %1, a[%c2:%c3]" :: "v"(c.Vf[db][ks]), "v"(c.Pk[qb][ks]), "i"(ao), "i"(ao + 15) : W4_ACLOB);
}
template <int X, int QB, int KS, int P> DI void pack1(W4C& c) {
  u32x4 w = __builtin_bit_cast(u32x4, c.Pk[QB][KS]);
  w[P] = cvtpk(c.S[X][KS >> 1][QB][8 * (KS & 1) + 2 * P], c.S[X][KS >> 1][QB][8 * (KS & 1) + 2 * P + 1]);
  c.Pk[QB][KS] = __builtin_bit_cast(bf16x8, w);
}
template <int X, int J> DI void rowsum1(W4C& c) {
  constexpr int qb = J >> 4, ks = (J >> 2) & 3, p = J & 3;
  c.rs0[qb] = fadd_s(c.rs0[qb], c.S[X][ks >> 1][qb][8 * (ks & 1) + 2 * p]); c.rs1[qb] = fadd_s(c.rs1[qb], c.S[X][ks >> 1][qb][8 * (ks & 1) + 2 * p + 1]);
}

template <int X, bool FIRST, int M, bool NEXTK = false, bool NEXTV = false> DI void gapA(W4C& c, unsigned vslot, const int t) {
  constexpr int Xp = X ^ 1;
  qk_mfma<X, FIRST, M>(c);
  if constexpr (M >= 16 && M <= 31) {
    const int tk = NEXTK ? t + 2 - c.T : t + 2, tv = NEXTV ? t + 1 - c.T : t + 1;
    constexpr int q = (M - 16) >> 1;
    if constexpr (((M - 16) & 1) == 0) {
      if constexpr (q < 4) c.pm0[q] = c.m0k + (unsigned)((t + 2) & 1) * 0x4000u + 1024u * q; else c.pm0[q] = c.m0v + (unsigned)((t + 1) & 1) * 0x4000u + 1024u * (q - 4);
      asm volatile("" : "+s"(c.pm0[q]));
    } else {
      if constexpr (q < 4) c.psf[q] = KTILEB * (unsigned)tk + c.sofk + KRS * 4u * q; else c.psf[q] = KTILEB * (unsigned)tv + c.sofv + KRS * 8u * ((q - 4) >> 1) + 128u * ((q - 4) & 1);
      asm volatile("" : "+s"(c.psf[q]));
    }
  }
  if constexpr (M == 31) asm volatile("s_nop 4" : "+s"(c.pm0[0]), "+s"(c.pm0[1]), "+s"(c.pm0[2]), "+s"(c.pm0[3]), "+s"(c.pm0[4]), "+s"(c.pm0[5]), "+s"(c.pm0[6]), "+s"(c.pm0[7]),
                                                  "+s"(c.psf[0]), "+s"(c.psf[1]), "+s"(c.psf[2]), "+s"(c.psf[3]), "+s"(c.psf[4]), "+s"(c.psf[5]), "+s"(c.psf[6]), "+s"(c.psf[7]));
  if constexpr (!FIRST && M == 8) asm volatile("" :: "v"(c.NEGM[0]), "v"(c.NEGM[1]));
  if constexpr (!FIRST) {
    constexpr int base = M + (M >= 4) + (M >= 8) + (M >= 12) + (M >= 16) + (M >= 20) + (M >= 24);
    if constexpr (M <= 25) tr_read<base>(c, vslot);
    if constexpr (M == 3 || M == 7 || M == 11 || M == 15 || M == 19 || M == 23) tr_read<base + 1>(c, vslot);
    if constexpr (M < 16) {
      constexpr int e0 = 2 * M, e1 = 2 * M + 1;
      c.S[Xp][1][e0 >> 4][e0 & 15] = __builtin_amdgcn_exp2f(c.S[Xp][1][e0 >> 4][e0 & 15]);
      c.S[Xp][1][e1 >> 4][e1 & 15] = __builtin_amdgcn_exp2f(c.S[Xp][1][e1 >> 4][e1 & 15]);
      pack1<Xp, (M >> 3), ((M >> 2) & 1), (M & 3)>(c);
    } else {
      constexpr int cc = M - 16;
      pack1<Xp, (cc >> 3), 2 + ((cc >> 2) & 1), (cc & 3)>(c);
      rowsum1<Xp, 16 * (cc >> 3) + 8 + 4 * ((cc >> 2) & 1) + (cc & 3)>(c);
    }
  }
  GAP();
}
template <int X, bool FIRST, bool NEXTK, bool NEXTV, int... Ms> DI void phaseA_(W4C& c, const int t, iseq<Ms...>) {
  const unsigned vslot = (unsigned)((t - 1) & 1) * 0x4000u;
  asm volatile("" : "+s"(c.sofk), "+s"(c.sofv), "+s"(c.m0k), "+s"(c.m0v));
  (gapA<X, FIRST, Ms, NEXTK, NEXTV>(c, vslot, t), ...);
  if constexpr (!FIRST) {
    #pragma unroll
    for (int qb = 0; qb < 2; ++qb) {
      const float rs = fadd_s(c.rs0[qb], c.rs1[qb]);
      auto pr = __builtin_amdgcn_permlane32_swap(__float_as_uint(rs), __float_as_uint(rs), false, false);
      c.lsum[qb] = fadd_s(c.lsum[qb], fadd_s(__uint_as_float(pr[0]), __uint_as_float(pr[1])));
    }
  }
#define W4_VLO "+v"(c.vlo[0][0]), "+v"(c.vlo[0][1]), "+v"(c.vlo[0][2]), "+v"(c.vlo[0][3]), "+v"(c.vlo[1][0]), "+v"(c.vlo[1][1]), "+v"(c.vlo[1][2]), "+v"(c.vlo[1][3]), \
               "+v"(c.vlo[2][0]), "+v"(c.vlo[2][1]), "+v"(c.vlo[2][2]), "+v"(c.vlo[2][3]), "+v"(c.vlo[3][0]), "+v"(c.vlo[3][1]), "+v"(c.vlo[3][2]), "+v"(c.vlo[3][3])
#define W4_VUP "+v"(c.vup[0][0]), "+v"(c.vup[0][1]), "+v"(c.vup[0][2]), "+v"(c.vup[0][3]), "+v"(c.vup[1][0]), "+v"(c.vup[1][1]), "+v"(c.vup[1][2]), "+v"(c.vup[1][3]), \
               "+v"(c.vup[2][0]), "+v"(c.vup[2][1]), "+v"(c.vup[2][2]), "+v"(c.vup[2][3]), "+v"(c.vup[3][0]), "+v"(c.vup[3][1]), "+v"(c.vup[3][2]), "+v"(c.vup[3][3])
  if constexpr (FIRST) asm volatile("s_waitcnt vmcnt(0) lgkmcnt(0)\n\ts_barrier" ::: "memory");
  else { asm volatile("s_waitcnt vmcnt(0) lgkmcnt(0)" : W4_VLO :: "memory"); asm volatile("s_barrier" : W4_VUP :: "memory"); }
  GAP();
  if constexpr (!FIRST) {
    #pragma unroll
    for (int ks = 0; ks < 4; ++ks)
      #pragma unroll
      for (int db = 0; db < 4; ++db) c.Vf[db][ks] = __builtin_shufflevector(c.vlo[db][ks], c.vup[db][ks], 0, 1, 2, 3, 4, 5, 6, 7);
  }
}
template <int X, bool FIRST, bool NEXTK = false, bool NEXTV = false> DI void phaseA(W4C& c, const int t) { phaseA_<X, FIRST, NEXTK, NEXTV>(c, t, make_iseq<32>{}); }

template <int X, bool PEEL, bool ZERO1, int DK, int DV, bool KR, int M> DI void gapB(W4C& c, const unsigned kslot_rd) {
  if constexpr (!PEEL) pv_mfma<M, ZERO1>(c);
  if constexpr (M < 4 && DK == 1)             asm volatile("s_mov_b32 m0, %0\n\ts_nop 0\n\tbuffer_load_dwordx4 %1, %2, %3 offen nt lds" :: "s"(c.pm0[M]), "v"(c.vksrc[M & 1]), "s"(c.srdK),  "s"(c.psf[M]) : "m0", "memory");
  else if constexpr (M < 4 && DK == 2)        asm volatile("s_mov_b32 m0, %0\n\ts_nop 0\n\tbuffer_load_dwordx4 %1, %2, %3 offen nt lds" :: "s"(c.pm0[M]), "v"(c.vksrc[M & 1]), "s"(c.srdKn), "s"(c.psf[M]) : "m0", "memory");
  else if constexpr (M >= 4 && M < 8 && DV == 1) asm volatile("s_mov_b32 m0, %0\n\ts_nop 0\n\tbuffer_load_dwordx4 %1, %2, %3 offen nt lds" :: "s"(c.pm0[M]), "v"(c.vvsrc), "s"(c.srdV),  "s"(c.psf[M]) : "m0", "memory");
  else if constexpr (M >= 4 && M < 8 && DV == 2) asm volatile("s_mov_b32 m0, %0\n\ts_nop 0\n\tbuffer_load_dwordx4 %1, %2, %3 offen nt lds" :: "s"(c.pm0[M]), "v"(c.vvsrc), "s"(c.srdVn), "s"(c.psf[M]) : "m0", "memory");
  if constexpr (M >= 1 && M <= 16 && KR) k_read<M - 1>(c, kslot_rd);
  if constexpr (M >= 8 && M < 16) {
    constexpr int i = M - 8, q = i >> 2, r = 4 * (i & 3);
    if constexpr ((i & 3) == 0) { c.pmx0[q] = max3f(c.S[X][0][q][0], c.S[X][0][q][1], c.S[X][1][q][0]); c.pmx1[q] = max3f(c.S[X][0][q][2], c.S[X][0][q][3], c.S[X][1][q][1]);
                                  c.pmx0[q] = max3f(c.pmx0[q], c.S[X][1][q][2], c.S[X][1][q][3]); }
    else                        { c.pmx0[q] = max3f(c.pmx0[q], c.S[X][0][q][r], c.S[X][0][q][r + 1]); c.pmx1[q] = max3f(c.pmx1[q], c.S[X][0][q][r + 2], c.S[X][0][q][r + 3]);
                                  c.pmx0[q] = max3f(c.pmx0[q], c.S[X][1][q][r], c.S[X][1][q][r + 1]); c.pmx1[q] = max3f(c.pmx1[q], c.S[X][1][q][r + 2], c.S[X][1][q][r + 3]); }
  }
  if constexpr (M == 16 || M == 17) {
    constexpr int q = M - 16; const float mm = max2f(c.pmx0[q], c.pmx1[q]);
    auto pr = __builtin_amdgcn_permlane32_swap(__float_as_uint(mm), __float_as_uint(mm), false, false);
    c.rmax[q] = max2f(__uint_as_float(pr[0]), __uint_as_float(pr[1]));
  }
  if constexpr (M == 18) {
    if constexpr (PEEL) {
      #pragma unroll
      for (int q = 0; q < 2; ++q) { c.mhat[q] = c.rmax[q]; c.lsum[q] = 0.f; c.fct[q] = 1.f;
        #pragma unroll
        for (int kb = 0; kb < 2; ++kb) { _Pragma("unroll") for (int e = 0; e < 16; ++e) c.S[X][kb][q][e] = fsub_s(c.S[X][kb][q][e], c.rmax[q]); }
        negm_bcast(c.NEGM[q], w4_aone(), -c.mhat[q]);
      }
    } else {
      const bool grew = (c.rmax[0] > c.THR) | (c.rmax[1] > c.THR);
      if (__builtin_expect(__any((int)grew), 0)) {
        #pragma unroll
        for (int q = 0; q < 2; ++q) { const float dl = max2f(c.rmax[q], 0.f); c.delta[q] = dl; c.mhat[q] = fadd_s(c.mhat[q], dl); c.fct[q] = __builtin_amdgcn_exp2f(-dl);
          #pragma unroll
          for (int kb = 0; kb < 2; ++kb) { _Pragma("unroll") for (int e = 0; e < 16; ++e) c.S[X][kb][q][e] = fsub_s(c.S[X][kb][q][e], dl); }
          negm_bcast(c.NEGM[q], w4_aone(), -c.mhat[q]);
        }
        c.pend = true;
      }
    }
  }
  if constexpr (M == 18) { c.rs0[0] = c.rs0[1] = c.rs1[0] = c.rs1[1] = 0.f; }
  if constexpr (M >= 19) {
    constexpr int g = M - 19, e_lo = (32 * g) / 13, e_hi = (32 * (g + 1)) / 13;
    {
      constexpr int p_lo = g ? (32 * (g - 1)) / 13 : 0, p_hi = g ? (32 * g) / 13 : 0;
      if constexpr (p_lo + 0 < p_hi) { constexpr int e = p_lo + 0; if constexpr (e & 1) c.rs1[e >> 4] = fadd_s(c.rs1[e >> 4], c.S[X][0][e >> 4][e & 15]); else c.rs0[e >> 4] = fadd_s(c.rs0[e >> 4], c.S[X][0][e >> 4][e & 15]); }
      if constexpr (p_lo + 1 < p_hi) { constexpr int e = p_lo + 1; if constexpr (e & 1) c.rs1[e >> 4] = fadd_s(c.rs1[e >> 4], c.S[X][0][e >> 4][e & 15]); else c.rs0[e >> 4] = fadd_s(c.rs0[e >> 4], c.S[X][0][e >> 4][e & 15]); }
      if constexpr (p_lo + 2 < p_hi) { constexpr int e = p_lo + 2; if constexpr (e & 1) c.rs1[e >> 4] = fadd_s(c.rs1[e >> 4], c.S[X][0][e >> 4][e & 15]); else c.rs0[e >> 4] = fadd_s(c.rs0[e >> 4], c.S[X][0][e >> 4][e & 15]); }
    }
    if constexpr (e_lo + 0 < e_hi) c.S[X][0][(e_lo + 0) >> 4][(e_lo + 0) & 15] = __builtin_amdgcn_exp2f(c.S[X][0][(e_lo + 0) >> 4][(e_lo + 0) & 15]);
    if constexpr (e_lo + 1 < e_hi) c.S[X][0][(e_lo + 1) >> 4][(e_lo + 1) & 15] = __builtin_amdgcn_exp2f(c.S[X][0][(e_lo + 1) >> 4][(e_lo + 1) & 15]);
    if constexpr (e_lo + 2 < e_hi) c.S[X][0][(e_lo + 2) >> 4][(e_lo + 2) & 15] = __builtin_amdgcn_exp2f(c.S[X][0][(e_lo + 2) >> 4][(e_lo + 2) & 15]);
  }
  if constexpr (M == 31) asm volatile("" :: "v"(c.S[X][0][0]), "v"(c.S[X][0][1]), "v"(c.NEGM[0]), "v"(c.NEGM[1]), "v"(c.rs0[0]), "v"(c.rs0[1]), "v"(c.rs1[0]), "v"(c.rs1[1]));
  GAP();
}
template <int X, bool PEEL, bool ZERO1, int DK, int DV, bool KR, int... Ms> DI void phaseB_(W4C& c, const int t, iseq<Ms...>) {
  const unsigned kslot_rd = (unsigned)((t + 1) & 1) * 0x4000u;
  if constexpr (!PEEL) asm volatile("s_nop 1" ::: "memory");
  GAP();
  (gapB<X, PEEL, ZERO1, DK, DV, KR, Ms>(c, kslot_rd), ...);
  asm volatile("s_nop 1"); GAP();
  {
    #pragma unroll
    for (int e = (32 * 12) / 13; e < 32; ++e) { if (e & 1) c.rs1[e >> 4] = fadd_s(c.rs1[e >> 4], c.S[X][0][e >> 4][e & 15]); else c.rs0[e >> 4] = fadd_s(c.rs0[e >> 4], c.S[X][0][e >> 4][e & 15]); }
  }
  if constexpr (!PEEL) {
    if (__builtin_expect(c.pend, 0)) {
      float t0, t1;
      asm volatile("s_nop 7\n\ts_nop 7\n\t"
        "v_accvgpr_read_b32 %0, a0\n\tv_mul_f32 %0, %2, %0\n\tv_accvgpr_write_b32 a0, %0\n\t"
        "v_accvgpr_read_b32 %1, a1\n\tv_mul_f32 %1, %2, %1\n\tv_accvgpr_write_b32 a1, %1\n\t"
        "v_accvgpr_read_b32 %0, a2\n\tv_mul_f32 %0, %2, %0\n\tv_accvgpr_write_b32 a2, %0\n\t"
        "v_accvgpr_read_b32 %1, a3\n\tv_mul_f32 %1, %2, %1\n\tv_accvgpr_write_b32 a3, %1\n\t"
        "v_accvgpr_read_b32 %0, a4\n\tv_mul_f32 %0, %2, %0\n\tv_accvgpr_write_b32 a4, %0\n\t"
        "v_accvgpr_read_b32 %1, a5\n\tv_mul_f32 %1, %2, %1\n\tv_accvgpr_write_b32 a5, %1\n\t"
        "v_accvgpr_read_b32 %0, a6\n\tv_mul_f32 %0, %2, %0\n\tv_accvgpr_write_b32 a6, %0\n\t"
        "v_accvgpr_read_b32 %1, a7\n\tv_mul_f32 %1, %2, %1\n\tv_accvgpr_write_b32 a7, %1\n\t"
        "v_accvgpr_read_b32 %0, a8\n\tv_mul_f32 %0, %2, %0\n\tv_accvgpr_write_b32 a8, %0\n\t"
        "v_accvgpr_read_b32 %1, a9\n\tv_mul_f32 %1, %2, %1\n\tv_accvgpr_write_b32 a9, %1\n\t"
        "v_accvgpr_read_b32 %0, a10\n\tv_mul_f32 %0, %2, %0\n\tv_accvgpr_write_b32 a10, %0\n\t"
        "v_accvgpr_read_b32 %1, a11\n\tv_mul_f32 %1, %2, %1\n\tv_accvgpr_write_b32 a11, %1\n\t"
        "v_accvgpr_read_b32 %0, a12\n\tv_mul_f32 %0, %2, %0\n\tv_accvgpr_write_b32 a12, %0\n\t"
        "v_accvgpr_read_b32 %1, a13\n\tv_mul_f32 %1, %2, %1\n\tv_accvgpr_write_b32 a13, %1\n\t"
        "v_accvgpr_read_b32 %0, a14\n\tv_mul_f32 %0, %2, %0\n\tv_accvgpr_write_b32 a14, %0\n\t"
        "v_accvgpr_read_b32 %1, a15\n\tv_mul_f32 %1, %2, %1\n\tv_accvgpr_write_b32 a15, %1\n\t"
        "v_accvgpr_read_b32 %0, a16\n\tv_mul_f32 %0, %3, %0\n\tv_accvgpr_write_b32 a16, %0\n\t"
        "v_accvgpr_read_b32 %1, a17\n\tv_mul_f32 %1, %3, %1\n\tv_accvgpr_write_b32 a17, %1\n\t"
        "v_accvgpr_read_b32 %0, a18\n\tv_mul_f32 %0, %3, %0\n\tv_accvgpr_write_b32 a18, %0\n\t"
        "v_accvgpr_read_b32 %1, a19\n\tv_mul_f32 %1, %3, %1\n\tv_accvgpr_write_b32 a19, %1\n\t"
        "v_accvgpr_read_b32 %0, a20\n\tv_mul_f32 %0, %3, %0\n\tv_accvgpr_write_b32 a20, %0\n\t"
        "v_accvgpr_read_b32 %1, a21\n\tv_mul_f32 %1, %3, %1\n\tv_accvgpr_write_b32 a21, %1\n\t"
        "v_accvgpr_read_b32 %0, a22\n\tv_mul_f32 %0, %3, %0\n\tv_accvgpr_write_b32 a22, %0\n\t"
        "v_accvgpr_read_b32 %1, a23\n\tv_mul_f32 %1, %3, %1\n\tv_accvgpr_write_b32 a23, %1\n\t"
        "v_accvgpr_read_b32 %0, a24\n\tv_mul_f32 %0, %3, %0\n\tv_accvgpr_write_b32 a24, %0\n\t"
        "v_accvgpr_read_b32 %1, a25\n\tv_mul_f32 %1, %3, %1\n\tv_accvgpr_write_b32 a25, %1\n\t"
        "v_accvgpr_read_b32 %0, a26\n\tv_mul_f32 %0, %3, %0\n\tv_accvgpr_write_b32 a26, %0\n\t"
        "v_accvgpr_read_b32 %1, a27\n\tv_mul_f32 %1, %3, %1\n\tv_accvgpr_write_b32 a27, %1\n\t"
        "v_accvgpr_read_b32 %0, a28\n\tv_mul_f32 %0, %3, %0\n\tv_accvgpr_write_b32 a28, %0\n\t"
        "v_accvgpr_read_b32 %1, a29\n\tv_mul_f32 %1, %3, %1\n\tv_accvgpr_write_b32 a29, %1\n\t"
        "v_accvgpr_read_b32 %0, a30\n\tv_mul_f32 %0, %3, %0\n\tv_accvgpr_write_b32 a30, %0\n\t"
        "v_accvgpr_read_b32 %1, a31\n\tv_mul_f32 %1, %3, %1\n\tv_accvgpr_write_b32 a31, %1\n\t"
        "v_accvgpr_read_b32 %0, a32\n\tv_mul_f32 %0, %2, %0\n\tv_accvgpr_write_b32 a32, %0\n\t"
        "v_accvgpr_read_b32 %1, a33\n\tv_mul_f32 %1, %2, %1\n\tv_accvgpr_write_b32 a33, %1\n\t"
        "v_accvgpr_read_b32 %0, a34\n\tv_mul_f32 %0, %2, %0\n\tv_accvgpr_write_b32 a34, %0\n\t"
        "v_accvgpr_read_b32 %1, a35\n\tv_mul_f32 %1, %2, %1\n\tv_accvgpr_write_b32 a35, %1\n\t"
        "v_accvgpr_read_b32 %0, a36\n\tv_mul_f32 %0, %2, %0\n\tv_accvgpr_write_b32 a36, %0\n\t"
        "v_accvgpr_read_b32 %1, a37\n\tv_mul_f32 %1, %2, %1\n\tv_accvgpr_write_b32 a37, %1\n\t"
        "v_accvgpr_read_b32 %0, a38\n\tv_mul_f32 %0, %2, %0\n\tv_accvgpr_write_b32 a38, %0\n\t"
        "v_accvgpr_read_b32 %1, a39\n\tv_mul_f32 %1, %2, %1\n\tv_accvgpr_write_b32 a39, %1\n\t"
        "v_accvgpr_read_b32 %0, a40\n\tv_mul_f32 %0, %2, %0\n\tv_accvgpr_write_b32 a40, %0\n\t"
        "v_accvgpr_read_b32 %1, a41\n\tv_mul_f32 %1, %2, %1\n\tv_accvgpr_write_b32 a41, %1\n\t"
        "v_accvgpr_read_b32 %0, a42\n\tv_mul_f32 %0, %2, %0\n\tv_accvgpr_write_b32 a42, %0\n\t"
        "v_accvgpr_read_b32 %1, a43\n\tv_mul_f32 %1, %2, %1\n\tv_accvgpr_write_b32 a43, %1\n\t"
        "v_accvgpr_read_b32 %0, a44\n\tv_mul_f32 %0, %2, %0\n\tv_accvgpr_write_b32 a44, %0\n\t"
        "v_accvgpr_read_b32 %1, a45\n\tv_mul_f32 %1, %2, %1\n\tv_accvgpr_write_b32 a45, %1\n\t"
        "v_accvgpr_read_b32 %0, a46\n\tv_mul_f32 %0, %2, %0\n\tv_accvgpr_write_b32 a46, %0\n\t"
        "v_accvgpr_read_b32 %1, a47\n\tv_mul_f32 %1, %2, %1\n\tv_accvgpr_write_b32 a47, %1\n\t"
        "v_accvgpr_read_b32 %0, a48\n\tv_mul_f32 %0, %3, %0\n\tv_accvgpr_write_b32 a48, %0\n\t"
        "v_accvgpr_read_b32 %1, a49\n\tv_mul_f32 %1, %3, %1\n\tv_accvgpr_write_b32 a49, %1\n\t"
        "v_accvgpr_read_b32 %0, a50\n\tv_mul_f32 %0, %3, %0\n\tv_accvgpr_write_b32 a50, %0\n\t"
        "v_accvgpr_read_b32 %1, a51\n\tv_mul_f32 %1, %3, %1\n\tv_accvgpr_write_b32 a51, %1\n\t"
        "v_accvgpr_read_b32 %0, a52\n\tv_mul_f32 %0, %3, %0\n\tv_accvgpr_write_b32 a52, %0\n\t"
        "v_accvgpr_read_b32 %1, a53\n\tv_mul_f32 %1, %3, %1\n\tv_accvgpr_write_b32 a53, %1\n\t"
        "v_accvgpr_read_b32 %0, a54\n\tv_mul_f32 %0, %3, %0\n\tv_accvgpr_write_b32 a54, %0\n\t"
        "v_accvgpr_read_b32 %1, a55\n\tv_mul_f32 %1, %3, %1\n\tv_accvgpr_write_b32 a55, %1\n\t"
        "v_accvgpr_read_b32 %0, a56\n\tv_mul_f32 %0, %3, %0\n\tv_accvgpr_write_b32 a56, %0\n\t"
        "v_accvgpr_read_b32 %1, a57\n\tv_mul_f32 %1, %3, %1\n\tv_accvgpr_write_b32 a57, %1\n\t"
        "v_accvgpr_read_b32 %0, a58\n\tv_mul_f32 %0, %3, %0\n\tv_accvgpr_write_b32 a58, %0\n\t"
        "v_accvgpr_read_b32 %1, a59\n\tv_mul_f32 %1, %3, %1\n\tv_accvgpr_write_b32 a59, %1\n\t"
        "v_accvgpr_read_b32 %0, a60\n\tv_mul_f32 %0, %3, %0\n\tv_accvgpr_write_b32 a60, %0\n\t"
        "v_accvgpr_read_b32 %1, a61\n\tv_mul_f32 %1, %3, %1\n\tv_accvgpr_write_b32 a61, %1\n\t"
        "v_accvgpr_read_b32 %0, a62\n\tv_mul_f32 %0, %3, %0\n\tv_accvgpr_write_b32 a62, %0\n\t"
        "v_accvgpr_read_b32 %1, a63\n\tv_mul_f32 %1, %3, %1\n\tv_accvgpr_write_b32 a63, %1\n\t"
        "v_accvgpr_read_b32 %0, a64\n\tv_mul_f32 %0, %2, %0\n\tv_accvgpr_write_b32 a64, %0\n\t"
        "v_accvgpr_read_b32 %1, a65\n\tv_mul_f32 %1, %2, %1\n\tv_accvgpr_write_b32 a65, %1\n\t"
        "v_accvgpr_read_b32 %0, a66\n\tv_mul_f32 %0, %2, %0\n\tv_accvgpr_write_b32 a66, %0\n\t"
        "v_accvgpr_read_b32 %1, a67\n\tv_mul_f32 %1, %2, %1\n\tv_accvgpr_write_b32 a67, %1\n\t"
        "v_accvgpr_read_b32 %0, a68\n\tv_mul_f32 %0, %2, %0\n\tv_accvgpr_write_b32 a68, %0\n\t"
        "v_accvgpr_read_b32 %1, a69\n\tv_mul_f32 %1, %2, %1\n\tv_accvgpr_write_b32 a69, %1\n\t"
        "v_accvgpr_read_b32 %0, a70\n\tv_mul_f32 %0, %2, %0\n\tv_accvgpr_write_b32 a70, %0\n\t"
        "v_accvgpr_read_b32 %1, a71\n\tv_mul_f32 %1, %2, %1\n\tv_accvgpr_write_b32 a71, %1\n\t"
        "v_accvgpr_read_b32 %0, a72\n\tv_mul_f32 %0, %2, %0\n\tv_accvgpr_write_b32 a72, %0\n\t"
        "v_accvgpr_read_b32 %1, a73\n\tv_mul_f32 %1, %2, %1\n\tv_accvgpr_write_b32 a73, %1\n\t"
        "v_accvgpr_read_b32 %0, a74\n\tv_mul_f32 %0, %2, %0\n\tv_accvgpr_write_b32 a74, %0\n\t"
        "v_accvgpr_read_b32 %1, a75\n\tv_mul_f32 %1, %2, %1\n\tv_accvgpr_write_b32 a75, %1\n\t"
        "v_accvgpr_read_b32 %0, a76\n\tv_mul_f32 %0, %2, %0\n\tv_accvgpr_write_b32 a76, %0\n\t"
        "v_accvgpr_read_b32 %1, a77\n\tv_mul_f32 %1, %2, %1\n\tv_accvgpr_write_b32 a77, %1\n\t"
        "v_accvgpr_read_b32 %0, a78\n\tv_mul_f32 %0, %2, %0\n\tv_accvgpr_write_b32 a78, %0\n\t"
        "v_accvgpr_read_b32 %1, a79\n\tv_mul_f32 %1, %2, %1\n\tv_accvgpr_write_b32 a79, %1\n\t"
        "v_accvgpr_read_b32 %0, a80\n\tv_mul_f32 %0, %3, %0\n\tv_accvgpr_write_b32 a80, %0\n\t"
        "v_accvgpr_read_b32 %1, a81\n\tv_mul_f32 %1, %3, %1\n\tv_accvgpr_write_b32 a81, %1\n\t"
        "v_accvgpr_read_b32 %0, a82\n\tv_mul_f32 %0, %3, %0\n\tv_accvgpr_write_b32 a82, %0\n\t"
        "v_accvgpr_read_b32 %1, a83\n\tv_mul_f32 %1, %3, %1\n\tv_accvgpr_write_b32 a83, %1\n\t"
        "v_accvgpr_read_b32 %0, a84\n\tv_mul_f32 %0, %3, %0\n\tv_accvgpr_write_b32 a84, %0\n\t"
        "v_accvgpr_read_b32 %1, a85\n\tv_mul_f32 %1, %3, %1\n\tv_accvgpr_write_b32 a85, %1\n\t"
        "v_accvgpr_read_b32 %0, a86\n\tv_mul_f32 %0, %3, %0\n\tv_accvgpr_write_b32 a86, %0\n\t"
        "v_accvgpr_read_b32 %1, a87\n\tv_mul_f32 %1, %3, %1\n\tv_accvgpr_write_b32 a87, %1\n\t"
        "v_accvgpr_read_b32 %0, a88\n\tv_mul_f32 %0, %3, %0\n\tv_accvgpr_write_b32 a88, %0\n\t"
        "v_accvgpr_read_b32 %1, a89\n\tv_mul_f32 %1, %3, %1\n\tv_accvgpr_write_b32 a89, %1\n\t"
        "v_accvgpr_read_b32 %0, a90\n\tv_mul_f32 %0, %3, %0\n\tv_accvgpr_write_b32 a90, %0\n\t"
        "v_accvgpr_read_b32 %1, a91\n\tv_mul_f32 %1, %3, %1\n\tv_accvgpr_write_b32 a91, %1\n\t"
        "v_accvgpr_read_b32 %0, a92\n\tv_mul_f32 %0, %3, %0\n\tv_accvgpr_write_b32 a92, %0\n\t"
        "v_accvgpr_read_b32 %1, a93\n\tv_mul_f32 %1, %3, %1\n\tv_accvgpr_write_b32 a93, %1\n\t"
        "v_accvgpr_read_b32 %0, a94\n\tv_mul_f32 %0, %3, %0\n\tv_accvgpr_write_b32 a94, %0\n\t"
        "v_accvgpr_read_b32 %1, a95\n\tv_mul_f32 %1, %3, %1\n\tv_accvgpr_write_b32 a95, %1\n\t"
        "v_accvgpr_read_b32 %0, a96\n\tv_mul_f32 %0, %2, %0\n\tv_accvgpr_write_b32 a96, %0\n\t"
        "v_accvgpr_read_b32 %1, a97\n\tv_mul_f32 %1, %2, %1\n\tv_accvgpr_write_b32 a97, %1\n\t"
        "v_accvgpr_read_b32 %0, a98\n\tv_mul_f32 %0, %2, %0\n\tv_accvgpr_write_b32 a98, %0\n\t"
        "v_accvgpr_read_b32 %1, a99\n\tv_mul_f32 %1, %2, %1\n\tv_accvgpr_write_b32 a99, %1\n\t"
        "v_accvgpr_read_b32 %0, a100\n\tv_mul_f32 %0, %2, %0\n\tv_accvgpr_write_b32 a100, %0\n\t"
        "v_accvgpr_read_b32 %1, a101\n\tv_mul_f32 %1, %2, %1\n\tv_accvgpr_write_b32 a101, %1\n\t"
        "v_accvgpr_read_b32 %0, a102\n\tv_mul_f32 %0, %2, %0\n\tv_accvgpr_write_b32 a102, %0\n\t"
        "v_accvgpr_read_b32 %1, a103\n\tv_mul_f32 %1, %2, %1\n\tv_accvgpr_write_b32 a103, %1\n\t"
        "v_accvgpr_read_b32 %0, a104\n\tv_mul_f32 %0, %2, %0\n\tv_accvgpr_write_b32 a104, %0\n\t"
        "v_accvgpr_read_b32 %1, a105\n\tv_mul_f32 %1, %2, %1\n\tv_accvgpr_write_b32 a105, %1\n\t"
        "v_accvgpr_read_b32 %0, a106\n\tv_mul_f32 %0, %2, %0\n\tv_accvgpr_write_b32 a106, %0\n\t"
        "v_accvgpr_read_b32 %1, a107\n\tv_mul_f32 %1, %2, %1\n\tv_accvgpr_write_b32 a107, %1\n\t"
        "v_accvgpr_read_b32 %0, a108\n\tv_mul_f32 %0, %2, %0\n\tv_accvgpr_write_b32 a108, %0\n\t"
        "v_accvgpr_read_b32 %1, a109\n\tv_mul_f32 %1, %2, %1\n\tv_accvgpr_write_b32 a109, %1\n\t"
        "v_accvgpr_read_b32 %0, a110\n\tv_mul_f32 %0, %2, %0\n\tv_accvgpr_write_b32 a110, %0\n\t"
        "v_accvgpr_read_b32 %1, a111\n\tv_mul_f32 %1, %2, %1\n\tv_accvgpr_write_b32 a111, %1\n\t"
        "v_accvgpr_read_b32 %0, a112\n\tv_mul_f32 %0, %3, %0\n\tv_accvgpr_write_b32 a112, %0\n\t"
        "v_accvgpr_read_b32 %1, a113\n\tv_mul_f32 %1, %3, %1\n\tv_accvgpr_write_b32 a113, %1\n\t"
        "v_accvgpr_read_b32 %0, a114\n\tv_mul_f32 %0, %3, %0\n\tv_accvgpr_write_b32 a114, %0\n\t"
        "v_accvgpr_read_b32 %1, a115\n\tv_mul_f32 %1, %3, %1\n\tv_accvgpr_write_b32 a115, %1\n\t"
        "v_accvgpr_read_b32 %0, a116\n\tv_mul_f32 %0, %3, %0\n\tv_accvgpr_write_b32 a116, %0\n\t"
        "v_accvgpr_read_b32 %1, a117\n\tv_mul_f32 %1, %3, %1\n\tv_accvgpr_write_b32 a117, %1\n\t"
        "v_accvgpr_read_b32 %0, a118\n\tv_mul_f32 %0, %3, %0\n\tv_accvgpr_write_b32 a118, %0\n\t"
        "v_accvgpr_read_b32 %1, a119\n\tv_mul_f32 %1, %3, %1\n\tv_accvgpr_write_b32 a119, %1\n\t"
        "v_accvgpr_read_b32 %0, a120\n\tv_mul_f32 %0, %3, %0\n\tv_accvgpr_write_b32 a120, %0\n\t"
        "v_accvgpr_read_b32 %1, a121\n\tv_mul_f32 %1, %3, %1\n\tv_accvgpr_write_b32 a121, %1\n\t"
        "v_accvgpr_read_b32 %0, a122\n\tv_mul_f32 %0, %3, %0\n\tv_accvgpr_write_b32 a122, %0\n\t"
        "v_accvgpr_read_b32 %1, a123\n\tv_mul_f32 %1, %3, %1\n\tv_accvgpr_write_b32 a123, %1\n\t"
        "v_accvgpr_read_b32 %0, a124\n\tv_mul_f32 %0, %3, %0\n\tv_accvgpr_write_b32 a124, %0\n\t"
        "v_accvgpr_read_b32 %1, a125\n\tv_mul_f32 %1, %3, %1\n\tv_accvgpr_write_b32 a125, %1\n\t"
        "v_accvgpr_read_b32 %0, a126\n\tv_mul_f32 %0, %3, %0\n\tv_accvgpr_write_b32 a126, %0\n\t"
        "v_accvgpr_read_b32 %1, a127\n\tv_mul_f32 %1, %3, %1\n\tv_accvgpr_write_b32 a127, %1\n\t"
        "s_nop 1" : "=&v"(t0), "=&v"(t1) : "v"(c.fct[0]), "v"(c.fct[1]) : W4_ACLOB);
      c.lsum[0] = fmul_s(c.lsum[0], c.fct[0]); c.lsum[1] = fmul_s(c.lsum[1], c.fct[1]); c.pend = false;
    }
  }
  asm volatile("s_waitcnt lgkmcnt(0)" ::: "memory");
  GAP();
}
template <int X, bool PEEL, bool ZERO1, int DK = 1, int DV = 1, bool KR = true> DI void phaseB(W4C& c, const int t) { phaseB_<X, PEEL, ZERO1, DK, DV, KR>(c, t, make_iseq<32>{}); }

template <int... Is> DI void k_reads(W4C& c, unsigned slot, iseq<Is...>) { (k_read<Is>(c, slot), ...); }
template <int... Is> DI void tr_reads(W4C& c, unsigned vslot, iseq<Is...>) { (tr_read<Is>(c, vslot), ...); }
template <int X, int... Cs> DI void packs(W4C& c, iseq<Cs...>) { (pack1<X, ((Cs >> 3) & 1), 2 * (Cs >> 4) + ((Cs >> 2) & 1), (Cs & 3)>(c), ...); }
template <int X, int J> DI void rowsum1_padded(W4C& c) { rowsum1<X, J>(c); asm volatile("s_nop 1"); }
template <int X, int... Js> DI void rowsums(W4C& c, iseq<Js...>) { (rowsum1_padded<X, Js>(c), ...); }
#define XSLOT 0x0000u
#define WRING 0x10000u
#define Q2PARK 0x10000u
constexpr unsigned WRS = 8192u;
constexpr int QNC = 10;
struct QPC {
  bf16x4 lo[4][4], up[4][4];
  bf16x8 xf[2][2][4];
  bf16x8 wf[4];
  unsigned vwb, wsrc[2], m0w, sofw;
  unsigned vxs, vxr[2], m0x, sofx;
  __amdgpu_buffer_rsrc_t srdW, srdX;
};
constexpr unsigned qp_wslot(int C, int HD) { return (unsigned)(C % 3) * 0x8000u + (unsigned)HD * 0x4000u; }
template <int KS, int I> DI void qp_tr(QPC& q, unsigned slot) {
  constexpr int db = I >> 1;
  if constexpr ((I & 1) == 0) asm volatile("ds_read_b64_tr_b16 %0, %1 offset:%2" : "=v"(q.lo[db][KS]) : "v"(q.vwb + slot), "i"(4096 * KS + 512 * db) : "memory");
  else                        asm volatile("ds_read_b64_tr_b16 %0, %1 offset:%2" : "=v"(q.up[db][KS]) : "v"(q.vwb + slot), "i"(4096 * KS + 2048 + 512 * db) : "memory");
}
template <int KS, int... Is> DI void qp_trs(QPC& q, unsigned slot, iseq<Is...>) { (qp_tr<KS, Is>(q, slot), ...); }
template <int C, int P> DI void qp_xload(QPC& q) {
  unsigned so;
  asm volatile("s_add_u32 m0, %1, %c5\n\ts_add_u32 %0, %4, %c6\n\tbuffer_load_dwordx4 %2, %3, %0 offen lds"
               : "=&s"(so) : "s"(q.m0x), "v"(q.vxs), "s"(q.srdX), "s"(q.sofx), "i"((C & 1) * 0x8000 + 1024 * P), "i"(1280 * 8 * P + 128 * C) : "m0", "scc", "memory");
}
template <int S, int I> DI void qp_xread(QPC& q) {
  constexpr int qb = I >> 2, ks = I & 3;
  asm volatile("ds_read_b128 %0, %1 offset:%c2" : "=v"(q.xf[S][qb][ks]) : "v"(q.vxr[ks & 1]), "i"(0x8000 * S + 4096 * qb + 64 * (ks >> 1)) : "memory");
}
template <int S, int... Is> DI void qp_xreads(QPC& q, iseq<Is...>) { (qp_xread<S, Is>(q), ...); }
template <int C, int P> DI void qp_dma(QPC& q) {
  unsigned so;
  asm volatile("s_add_u32 m0, %1, %c5\n\ts_add_u32 %0, %4, %c6\n\tbuffer_load_dwordx4 %2, %3, %0 offen lds"
               : "=&s"(so) : "s"(q.m0w), "v"(q.wsrc[(P >> 1) & 1]), "s"(q.srdW), "s"(q.sofw), "i"((int)(qp_wslot(C, P >> 2) + 1024u * (P & 3))),
                 "i"((int)(WRS * 64u * C + 256u * (P >> 2) + 128u * (P & 1))) : "m0", "scc", "memory");
}
template <int C, int... Is> DI void qp_xloads(QPC& q, iseq<Is...>) { (qp_xload<C, Is>(q), ...); }
template <int C, int... Is> DI void qp_dmas(QPC& q, iseq<Is...>) { (qp_dma<C, Is>(q), ...); }
#define QP_G(K) "+v"(q.lo[0][K]), "+v"(q.lo[1][K]), "+v"(q.lo[2][K]), "+v"(q.lo[3][K]), "+v"(q.up[0][K]), "+v"(q.up[1][K]), "+v"(q.up[2][K]), "+v"(q.up[3][K])
#define QP_X(S) "+v"(q.xf[S][0][0]), "+v"(q.xf[S][0][1]), "+v"(q.xf[S][0][2]), "+v"(q.xf[S][0][3]), "+v"(q.xf[S][1][0]), "+v"(q.xf[S][1][1]), "+v"(q.xf[S][1][2]), "+v"(q.xf[S][1][3])
constexpr int qp_wait(int C) {
  int pos = 0, last = -1;
  auto ev = [&](int n, bool need) { for (int i = 0; i < n; ++i) { if (need) last = pos; ++pos; } };
  ev(8, false); ev(8, C + 1 == 1); ev(8, false); ev(8, false);
  for (int c = 0; c <= C; ++c) {
    if (c + 2 < QNC) { ev(8, c == C); ev(8, c + 2 == C + 1); }
  }
  return last < 0 ? 63 : pos - 1 - last;
}
template <int C, int HD, int M> DI void qp_gap(QPC& q) {
  constexpr int ks = M >> 3, db = (M >> 1) & 3, qb = M & 1, ao = 128 * HD + AO_(db, qb);
  constexpr bool LAST = (C == QNC - 1 && HD == 1);
  if constexpr ((M & 7) == 0) {
    if constexpr (ks == 0 && HD == 0) { asm volatile("s_waitcnt lgkmcnt(0)" : QP_G(0) :: "memory");
                             if constexpr ((C & 1) == 0) asm volatile("" : QP_X(0) :: "memory"); else asm volatile("" : QP_X(1) :: "memory"); }
    else if constexpr (LAST && ks >= 2) asm volatile("s_waitcnt lgkmcnt(0)" : QP_G(ks) :: "memory");
    else                                asm volatile("s_waitcnt lgkmcnt(8)" : QP_G(ks) :: "memory");
    #pragma unroll
    for (int d = 0; d < 4; ++d) q.wf[d] = __builtin_shufflevector(q.lo[d][ks], q.up[d][ks], 0, 1, 2, 3, 4, 5, 6, 7);
    asm volatile("s_nop 1" : "+v"(q.wf[0]), "+v"(q.wf[1]), "+v"(q.wf[2]), "+v"(q.wf[3]));
    GAP();
  }
  if constexpr (C == 0 && ks == 0) asm volatile("v_mfma_f32_32x32x16_bf16 a[%c2:%c3], %0, %1, 0" :: "v"(q.wf[db]), "v"(q.xf[C & 1][qb][ks]), "i"(ao), "i"(ao + 15) : W4_ACLOB, W4_ACLOB_HI);
  else                             asm volatile("v_mfma_f32_32x32x16_bf16 a[%c2:%c3], %0, %1, a[%c2:%c3]" :: "v"(q.wf[db]), "v"(q.xf[C & 1][qb][ks]), "i"(ao), "i"(ao + 15) : W4_ACLOB, W4_ACLOB_HI);
  if constexpr (ks == 0) qp_tr<3, M & 7>(q, qp_wslot(C, HD));
  else if constexpr (HD == 0) qp_tr<ks - 1, M & 7>(q, qp_wslot(C, 1));
  else if constexpr (!LAST) qp_tr<ks - 1, M & 7>(q, qp_wslot(C + 1, 0));
  if constexpr (C + 2 < QNC && (M & 3) == 1) {
    if constexpr (HD == 0) qp_dma<C + 2, (M >> 2)>(q); else qp_xload<C + 2, (M >> 2)>(q);
  }
  GAP();
}
template <int C, int HD, int... Ms> DI void qp_gaps(QPC& q, iseq<Ms...>) { (qp_gap<C, HD, Ms>(q), ...); }
template <int C> DI void qp_chunk(QPC& q) {
  qp_gaps<C, 0>(q, make_iseq<32>{});
  qp_gaps<C, 1>(q, make_iseq<32>{});
  if constexpr (C + 1 < QNC) {
    constexpr int W = qp_wait(C);
    asm volatile("s_waitcnt vmcnt(%0)\n\ts_barrier" :: "i"(W) : "memory");
    qp_xreads<(C + 1) & 1>(q, make_iseq<8>{});
    GAP();
    if constexpr (C + 2 < QNC) qp_chunk<C + 1>(q);
  }
}
template <int B> DI void acc_read16(float (&o)[16]) {
#define RD4(i) asm volatile("v_accvgpr_read_b32 %0, a[%c4]\n\tv_accvgpr_read_b32 %1, a[%c5]\n\tv_accvgpr_read_b32 %2, a[%c6]\n\tv_accvgpr_read_b32 %3, a[%c7]" \
    : "=v"(o[i]), "=v"(o[i + 1]), "=v"(o[i + 2]), "=v"(o[i + 3]) : "i"(B + i), "i"(B + i + 1), "i"(B + i + 2), "i"(B + i + 3))
  RD4(0); RD4(4); RD4(8); RD4(12);
#undef RD4
}
DI u32x4 frag_pack(const float (&o)[16], int m, float sc) {
  const unsigned ax = cvtpk(o[8 * m + 0] * sc, o[8 * m + 1] * sc), ay = cvtpk(o[8 * m + 2] * sc, o[8 * m + 3] * sc);
  const unsigned bx = cvtpk(o[8 * m + 4] * sc, o[8 * m + 5] * sc), by = cvtpk(o[8 * m + 6] * sc, o[8 * m + 7] * sc);
  auto rx = __builtin_amdgcn_permlane32_swap(ax, bx, false, false);
  auto ry = __builtin_amdgcn_permlane32_swap(ay, by, false, false);
  u32x4 w = {rx[0], ry[0], rx[1], ry[1]};
  return w;
}
template <int I> DI void q2_cvt(char* park, float sc) {
  float o[16]; acc_read16<128 + 16 * I>(o);
  #pragma unroll
  for (int m = 0; m < 2; ++m) *(u32x4*)(park + ((I & 1) * 8 + (I >> 1) * 2 + m) * 1024) = frag_pack(o, m, sc);
}
template <int I> DI void q1_cvt(float sc) {
  float o[16]; acc_read16<16 * I>(o);
  #pragma unroll
  for (int m = 0; m < 2; ++m) { u32x4 w = frag_pack(o, m, sc); asm volatile("s_nop 1" : "+v"(w)); q_write(I & 1, (I >> 1) * 2 + m, w); }
}
template <int... Is> DI void q2_cvts(char* park, float sc, iseq<Is...>) { (q2_cvt<Is>(park, sc), ...); }
template <int... Is> DI void q1_cvts(float sc, iseq<Is...>) { (q1_cvt<Is>(sc), ...); }
template <int M> DI void pv_drain1(W4C& c, unsigned vq2) {
  pv_mfma<M, false>(c);
  if constexpr (M & 1) {
    constexpr int qp = M >> 1, aq = AQ_(qp >> 3, qp & 7);
    asm volatile("ds_read_b128 a[%c1:%c2], %0 offset:%c3" :: "v"(vq2), "i"(aq), "i"(aq + 3), "i"(qp * 1024) : "memory", W4_ACLOB_HI);
  }
  GAP();
}
template <int... Ms> DI void pv_drain(W4C& c, unsigned vq2, iseq<Ms...>) { (pv_drain1<Ms>(c, vq2), ...); }

__global__ __launch_bounds__(256, 1)
void attn_pw(const unsigned short* __restrict__ xA, const unsigned short* __restrict__ kA, const unsigned short* __restrict__ vA, const unsigned short* __restrict__ wqA,
             const unsigned short* __restrict__ xB, const unsigned short* __restrict__ kB, const unsigned short* __restrict__ vB, const unsigned short* __restrict__ wqB,
             unsigned short* __restrict__ ows) {
  extern __shared__ __attribute__((aligned(16))) char lds[];
  const unsigned tid = threadIdx.x, lane = tid & 63u, wid = tid >> 6, l32 = lane & 31u, hi = lane >> 5;
  constexpr int T = 16;
  constexpr unsigned KVBYTES = 1024u * 256u;
  W4C c;
  c.T = T; c.THR = 8.0f;
  c.ldsb = (unsigned)(size_t)lds;
  #pragma unroll
  for (int j = 0; j < 4; ++j) c.vkb[j] = c.ldsb + KRING + 256u * l32 + 16u * ((2u * j + hi) ^ (l32 & 7u));
  c.vvb = c.ldsb + VRING + (((lane & 3u) << 3) | (((lane >> 2) & 3u) << 6) | (((lane >> 4) & 1u) << 5) | (hi << 8));
  #pragma unroll
  for (int par = 0; par < 2; ++par) c.vksrc[par] = KRS * (lane >> 4) + 16u * ((lane & 15u) ^ (4u * par + (lane >> 4)));
  c.vvsrc = KRS * ((lane >> 2) & 7u) + 64u * hi + 16u * (lane & 3u);
  c.m0k = rfl(c.ldsb + KRING + 4096u * wid); c.m0v = rfl(c.ldsb + VRING + 4096u * wid);
  c.sofk = rfl(KRS * 16u * wid); c.sofv = rfl(KRS * 16u * wid);

  const unsigned xcd = blockIdx.x & 7u, xi = blockIdx.x >> 3;
  const unsigned wblk = xcd >> 2, wpair = (xcd & 3u) * 4u + (xi & 3u), wbat = xi >> 2;
  const unsigned item0 = wblk * 256u + wbat * 32u + 2u * wpair;
  for (unsigned ublk = item0; ublk < item0 + 2u; ++ublk) {
    unsigned lb;
    asm volatile("v_mbcnt_lo_u32_b32 %0, -1, 0\n\tv_mbcnt_hi_u32_b32 %0, -1, %0" : "=v"(lb));
    const unsigned bh = ublk & 255u;
    const char* Kbase = (const char*)((ublk >> 8) ? kB : kA) + (size_t)bh * KVBYTES;
    const char* Vbase = (const char*)((ublk >> 8) ? vB : vA) + (size_t)bh * KVBYTES;
    c.srdK = __builtin_amdgcn_make_buffer_rsrc((void*)Kbase, (short)0, (int)KVBYTES, SRD_WORD3);
    c.srdV = __builtin_amdgcn_make_buffer_rsrc((void*)Vbase, (short)0, (int)KVBYTES, SRD_WORD3);
    auto dma_k = [&](int tk) {
      const unsigned slot = (unsigned)(tk & 1) * 0x4000u, skv = KTILEB * (unsigned)tk;
      #pragma unroll
      for (int q = 0; q < 4; ++q)
        asm volatile("s_nop 4\n\ts_mov_b32 m0, %0\n\ts_nop 0\n\tbuffer_load_dwordx4 %1, %2, %3 offen nt lds"
                     :: "s"(rfl(c.m0k + slot + 1024u * q)), "v"(c.vksrc[q & 1]), "s"(c.srdK), "s"(rfl(skv + c.sofk + KRS * 4u * q)) : "m0", "memory");
    };
    auto dma_v = [&](int tv) {
      const unsigned slot = (unsigned)(tv & 1) * 0x4000u, skv = KTILEB * (unsigned)tv;
      #pragma unroll
      for (int q = 0; q < 4; ++q)
        asm volatile("s_nop 4\n\ts_mov_b32 m0, %0\n\ts_nop 0\n\tbuffer_load_dwordx4 %1, %2, %3 offen nt lds"
                     :: "s"(rfl(c.m0v + slot + 1024u * q)), "v"(c.vvsrc), "s"(c.srdV), "s"(rfl(skv + c.sofv + KRS * 8u * (q >> 1) + 128u * (q & 1))) : "m0", "memory");
    };
    const bool warm = (ublk != item0);
    if (!warm) {
      QPC q;
      {
        const unsigned l32b = lb & 31u, hib = lb >> 5, r8 = (lb >> 2) & 7u, rr = lb >> 3;
        q.vwb = c.ldsb + WRING + (((lb & 3u) << 3) | (((lb >> 2) & 3u) << 6) | (((lb >> 4) & 1u) << 5) | (hib << 8));
        #pragma unroll
        for (int gp = 0; gp < 2; ++gp) q.wsrc[gp] = WRS * (8u * (r8 >> 2) + 4u * gp + (r8 & 3u)) + 64u * hib + 16u * (lb & 3u);
        q.m0w = rfl(c.ldsb + WRING + 4096u * wid); q.sofw = rfl(WRS * 16u * wid);
        q.srdX = __builtin_amdgcn_make_buffer_rsrc((void*)((const char*)(wblk ? xB : xA) + (size_t)wbat * (240u * 1280u)), (short)0, (int)(240u * 1280u), SRD_WORD3);
        q.vxs = 1280u * rr + 16u * ((lb & 7u) ^ ((rr >> 1) & 3u));
        q.m0x = rfl(c.ldsb + XSLOT + 8192u * wid); q.sofx = rfl(1280u * 64u * wid);
        #pragma unroll
        for (int j = 0; j < 2; ++j) q.vxr[j] = c.ldsb + XSLOT + 8192u * wid + 128u * l32b + 16u * ((2u * j + hib) ^ ((l32b >> 1) & 3u));
        q.srdW = __builtin_amdgcn_make_buffer_rsrc((void*)((const char*)(wblk ? wqB : wqA) + wpair * 512u), (short)0, (int)(640u * WRS - wpair * 512u), SRD_WORD3);
      }
      asm volatile("s_nop 4" ::: "memory");
      qp_xloads<0>(q, make_iseq<8>{}); qp_xloads<1>(q, make_iseq<8>{});
      qp_dmas<0>(q, make_iseq<8>{}); qp_dmas<1>(q, make_iseq<8>{});
      asm volatile("s_waitcnt vmcnt(0)\n\ts_barrier" ::: "memory");
      qp_xreads<0>(q, make_iseq<8>{});
      qp_trs<0>(q, 0u, make_iseq<8>{}); qp_trs<1>(q, 0u, make_iseq<8>{}); qp_trs<2>(q, 0u, make_iseq<8>{});
      asm volatile("s_waitcnt lgkmcnt(0)" ::: "memory");
      GAP();
      qp_chunk<0>(q);
      dma_k(0); dma_k(1);
      GAP();
      qp_chunk<QNC - 1>(q);
      asm volatile("s_barrier" ::: "memory");
      dma_v(0);
      asm volatile("s_nop 7\n\ts_nop 7" ::: "memory");
      const float C = 0.12751743082f;
      q2_cvts(lds + Q2PARK + 16384u * wid + 16u * lb, C, make_iseq<8>{});
      q1_cvts(C, make_iseq<8>{});
    }
    asm volatile("s_waitcnt vmcnt(0)\n\ts_barrier" ::: "memory");
    if (!warm) k_reads(c, 0u, make_iseq<16>{});
    asm volatile("s_waitcnt lgkmcnt(0)\n\ts_nop 3" ::: "memory");

    c.NEGM[0] = f32x16{}; c.NEGM[1] = f32x16{};
    c.mhat[0] = c.mhat[1] = 0.f; c.lsum[0] = c.lsum[1] = 0.f; c.fct[0] = c.fct[1] = 1.f; c.pend = false;

    phaseA<0, true>(c, 0);  phaseB<0, true,  false>(c, 0);
    phaseA<1, false>(c, 1); phaseB<1, false, true>(c, 1);
    for (int t = 2; t < T - 2; t += 2) {
      phaseA<0, false>(c, t);     phaseB<0, false, false>(c, t);
      phaseA<1, false>(c, t + 1); phaseB<1, false, false>(c, t + 1);
    }
    {
      const unsigned bhn = (ublk + 1u) & 255u; const int nrec = warm ? 0 : (int)KVBYTES;
      c.srdKn = __builtin_amdgcn_make_buffer_rsrc((void*)((const char*)(wblk ? kB : kA) + (size_t)bhn * KVBYTES), (short)0, nrec, SRD_WORD3);
      c.srdVn = __builtin_amdgcn_make_buffer_rsrc((void*)((const char*)(wblk ? vB : vA) + (size_t)bhn * KVBYTES), (short)0, nrec, SRD_WORD3);
    }
    phaseA<0, false, true, false>(c, T - 2); phaseB<0, false, false, 2, 1, true>(c, T - 2);
    phaseA<1, false, true, true >(c, T - 1); phaseB<1, false, false, 2, 2, true>(c, T - 1);
    {
      const unsigned vslot = (unsigned)((T - 1) & 1) * 0x4000u;
      tr_reads(c, vslot, make_iseq<32>{});
      #pragma unroll
      for (int e = 0; e < 32; ++e) c.S[1][1][e >> 4][e & 15] = __builtin_amdgcn_exp2f(c.S[1][1][e >> 4][e & 15]);
      packs<1>(c, make_iseq<32>{});
      GAP(); asm volatile("s_nop 1"); GAP();
      rowsums<1>(c, iseq<8, 9, 10, 11, 12, 13, 14, 15, 24, 25, 26, 27, 28, 29, 30, 31>{});
      #pragma unroll
      for (int qb = 0; qb < 2; ++qb) { const float rs = fadd_s(c.rs0[qb], c.rs1[qb]);
        auto pr = __builtin_amdgcn_permlane32_swap(__float_as_uint(rs), __float_as_uint(rs), false, false);
        c.lsum[qb] = fadd_s(c.lsum[qb], fadd_s(__uint_as_float(pr[0]), __uint_as_float(pr[1]))); }
      asm volatile("s_waitcnt lgkmcnt(0)" : W4_VLO :: "memory"); asm volatile("" : W4_VUP :: "memory");
      GAP();
      #pragma unroll
      for (int ks = 0; ks < 4; ++ks)
        #pragma unroll
        for (int db = 0; db < 4; ++db) c.Vf[db][ks] = __builtin_shufflevector(c.vlo[db][ks], c.vup[db][ks], 0, 1, 2, 3, 4, 5, 6, 7);
      asm volatile("s_nop 1" ::: "memory");
      pv_drain(c, c.ldsb + Q2PARK + 16384u * wid + 16u * lb, make_iseq<32>{});
      asm volatile("s_nop 7\n\ts_nop 7" ::: "memory");
    }
    {
      #pragma unroll
      for (int qb = 0; qb < 2; ++qb) {
        const float il = __builtin_amdgcn_rcpf(c.lsum[qb]);
        char* obase = (char*)ows + ((((size_t)(ublk >> 5) * 8u + 2u * wid + qb) * 256u + (ublk & 31u) * 8u) * 1024u);
        #pragma unroll
        for (int db = 0; db < 4; ++db) {
          float o[16];
          W4_READ_O(db, qb, o);
          #pragma unroll
          for (int m = 0; m < 2; ++m) {
            const unsigned ax = cvtpk(o[8 * m + 0] * il, o[8 * m + 1] * il), ay = cvtpk(o[8 * m + 2] * il, o[8 * m + 3] * il);
            const unsigned bx = cvtpk(o[8 * m + 4] * il, o[8 * m + 5] * il), by = cvtpk(o[8 * m + 6] * il, o[8 * m + 7] * il);
            auto rx = __builtin_amdgcn_permlane32_swap(ax, bx, false, false);
            auto ry = __builtin_amdgcn_permlane32_swap(ay, by, false, false);
            u32x4 w = {rx[0], ry[0], rx[1], ry[1]};
            *(u32x4*)(obase + ((unsigned)(db * 2 + m) * 1024u + lb * 16u)) = w;
          }
        }
      }
    }
  }
}
}

typedef float f32x4 __attribute__((ext_vector_type(4)));
constexpr int OL_NST = 6, OL_NCH = 32, OL_AB = 16384, OL_STG = OL_AB + 10240, OL_SHM = OL_NST * OL_STG;
template <int OFF> __device__ __forceinline__ bf16x8 lds_rd16(int vb) {
  bf16x8 r; asm volatile("ds_read_b128 %0, %1 offset:%2" : "=&v"(r) : "v"(vb), "i"(OFF) : "memory"); return r;
}
struct OLC {
  f32x16 acc[2][5];
  bf16x8 fa[2][2];
  s16x4 fb[2][10];
  int va, vb;
};
template <int R, int KS, int I> __device__ __forceinline__ void ol_read(OLC& c, int st) {
  if constexpr (I < 2) c.fa[R][I] = lds_rd16<(2 * I + KS) * 1024>(c.va + st);
  else { constexpr int nb = (I - 2) >> 1, h = (I - 2) & 1; c.fb[R][I - 2] = tr_read<((2 * KS + h) * 5 + nb) * 512>(c.vb + st); }
}
#define OL_TIE(R) "+v"(c.fa[R][0]), "+v"(c.fa[R][1]), "+v"(c.fb[R][0]), "+v"(c.fb[R][1]), "+v"(c.fb[R][2]), "+v"(c.fb[R][3]), "+v"(c.fb[R][4]), \
                  "+v"(c.fb[R][5]), "+v"(c.fb[R][6]), "+v"(c.fb[R][7]), "+v"(c.fb[R][8]), "+v"(c.fb[R][9])
template <int R, int KSN, bool RD, bool BAR, int J> __device__ __forceinline__ void ol_gap(OLC& c, int stn) {
  constexpr int nb = J >> 1, rb = J & 1;
  c.acc[rb][nb] = __builtin_amdgcn_mfma_f32_32x32x16_bf16(PKLH(c.fb[R][2 * nb], c.fb[R][2 * nb + 1]), c.fa[R][rb], c.acc[rb][nb], 0, 0, 0);
  SBAR();
  if constexpr (RD && J < 6) { ol_read<R ^ 1, KSN, 2 * J>(c, stn); ol_read<R ^ 1, KSN, 2 * J + 1>(c, stn); }
  if constexpr (BAR && J == 6) BARRIER();
  SBAR();
  if constexpr (J < 9) ol_gap<R, KSN, RD, BAR, J + 1>(c, stn);
}
template <int R, int KSN, bool RD, bool BAR> __device__ __forceinline__ void ol_step(OLC& c, int stn) {
  if constexpr (R == 0) asm volatile("s_waitcnt lgkmcnt(0)" : OL_TIE(0) :: "memory"); else asm volatile("s_waitcnt lgkmcnt(0)" : OL_TIE(1) :: "memory");
  SBAR();
  ol_gap<R, KSN, RD, BAR, 0>(c, stn);
}
template <int NBP> __device__ __forceinline__ void ol_loader(const u16* ap, const u16* bp, const int (&b_src)[3], int lda, int ldbb) {
  auto issue = [&](int ch, int st) {
#pragma unroll
    for (int e = 0; e < 4; ++e) dma16(ap + (size_t)(e >> 1) * (256 * 512) + (ch * 2 + (e & 1)) * 512, lda + st + e * 1024);
#pragma unroll
    for (int e = 0; e < NBP; ++e) dma16(bp + (size_t)ch * (32 * DM) + b_src[e], ldbb + st + e * 1024);
  };
  constexpr int NP = 4 + NBP;
#pragma unroll
  for (int ch = 0; ch < OL_NST; ++ch) issue(ch, ch * OL_STG);
  waitvm<4 * NP>(); BARRIER();
  int st = 0;
#pragma unroll 1
  for (int ch = 0; ch < OL_NCH - OL_NST; ++ch) {
    waitvm<3 * NP>(); BARRIER();
    issue(ch + OL_NST, st);
    st += OL_STG; if (st == OL_NST * OL_STG) st = 0;
  }
  waitvm<3 * NP>(); BARRIER();
  waitvm<2 * NP>(); BARRIER();
  waitvm<1 * NP>(); BARRIER();
  waitvm<0>();      BARRIER();
}
__global__ __launch_bounds__(512, 1)
void oproj_lc(const u16* __restrict__ ows, const u16* __restrict__ woA, const u16* __restrict__ woB, float* __restrict__ pws) {
  extern __shared__ __attribute__((aligned(16))) char lds[];
  const int bid = blockIdx.x, xcd = bid & 7, ii = bid >> 3, blk = xcd >> 2, sk = xcd & 3, nt = ii & 3, b = ii >> 2;
  const int tid = threadIdx.x, wid = __builtin_amdgcn_readfirstlane(tid >> 6), lane = tid & 63;
  const int lds0 = (int)(uintptr_t)lds;
  if (wid >= 4) {
    const int lw = wid - 4, pb = lw < 2 ? 3 * lw : 2 * lw + 2;
    int b_src[3];
#pragma unroll
    for (int e = 0; e < 3; ++e) {
      const int sub = 2 * (pb + e) + (lane >> 5), kg = sub / 5, cb = sub % 5;
      const int kk = kg * 8 + ((lane & 31) >> 2), k = (kk & ~0xC) | ((kk & 4) << 1) | ((kk & 8) >> 1);
      b_src[e] = k * DM + cb * 32 + (lane & 3) * 8;
    }
    const u16* ap = ows + ((((size_t)blk * NBATCH + b) * 8 + 2 * lw) * 256 + sk * 64) * 512 + lane * 8;
    const u16* bp = (blk ? woB : woA) + (size_t)sk * 1024 * DM + nt * 160;
    if (lw < 2) ol_loader<3>(ap, bp, b_src, lds0 + lw * 4096, lds0 + OL_AB + pb * 1024);
    else        ol_loader<2>(ap, bp, b_src, lds0 + lw * 4096, lds0 + OL_AB + pb * 1024);
    return;
  }
  OLC c;
#pragma unroll
  for (int rb = 0; rb < 2; ++rb)
#pragma unroll
    for (int nb = 0; nb < 5; ++nb) c.acc[rb][nb] = f32x16{};
  c.va = lds0 + wid * 4096 + lane * 16;
  c.vb = lds0 + OL_AB + v_rd_base(lane);
  BARRIER(); SBAR();
  ol_read<0, 0, 0>(c, 0); ol_read<0, 0, 1>(c, 0); ol_read<0, 0, 2>(c, 0); ol_read<0, 0, 3>(c, 0); ol_read<0, 0, 4>(c, 0); ol_read<0, 0, 5>(c, 0);
  ol_read<0, 0, 6>(c, 0); ol_read<0, 0, 7>(c, 0); ol_read<0, 0, 8>(c, 0); ol_read<0, 0, 9>(c, 0); ol_read<0, 0, 10>(c, 0); ol_read<0, 0, 11>(c, 0);
  int st = 0;
#pragma unroll 1
  for (int ch = 0; ch < OL_NCH - 2; ++ch) {
    int stn = st + OL_STG; if (stn == OL_NST * OL_STG) stn = 0;
    ol_step<0, 1, true, false>(c, st);
    ol_step<1, 0, true, true>(c, stn);
    st = stn;
  }
  { int stn = st + OL_STG; if (stn == OL_NST * OL_STG) stn = 0;
    ol_step<0, 1, true, false>(c, st);
    ol_step<1, 0, true, false>(c, stn);
    ol_step<0, 1, true, false>(c, stn);
    ol_step<1, 0, false, false>(c, stn); }
  float* po = pws + ((((size_t)sk * 2 + blk) * NBATCH + b) * 4 + nt) * (256 * 160) + wid * (64 * 160) + lane * 4;
#pragma unroll
  for (int rb = 0; rb < 2; ++rb)
#pragma unroll
    for (int nb = 0; nb < 5; ++nb)
#pragma unroll
      for (int g = 0; g < 4; ++g) {
        f32x4 w = {c.acc[rb][nb][4 * g + 0], c.acc[rb][nb][4 * g + 1], c.acc[rb][nb][4 * g + 2], c.acc[rb][nb][4 * g + 3]};
        *reinterpret_cast<f32x4*>(po + ((rb * 5 + nb) * 4 + g) * 256) = w;
      }
}
__global__ __launch_bounds__(256)
void oreduce2(const float* __restrict__ pws, u16* __restrict__ out) {
  const int t = blockIdx.x * 256 + threadIdx.x;
  const int g = t & 3, r16 = (t >> 2) & 15, rh = (t >> 6) & 1, u = t >> 7;
  const int nb = u % 5, wrb = (u / 5) & 7, nt = (u / 40) & 3, bb = u / 160, b = bb & 7, blk = bb >> 3;
  const int r32 = rh * 16 + r16, row = wrb * 32 + r32;
  if (row >= SQ) return;
  const float* p = pws + ((size_t)u * 4 + g) * 256 + r32 * 4;
  constexpr size_t SS = (size_t)2 * NBATCH * 4 * 256 * 160;
  f32x4 a0 = *reinterpret_cast<const f32x4*>(p), a1 = *reinterpret_cast<const f32x4*>(p + 128);
#pragma unroll
  for (int s = 1; s < 4; ++s) { a0 += *reinterpret_cast<const f32x4*>(p + s * SS); a1 += *reinterpret_cast<const f32x4*>(p + s * SS + 128); }
  u32x4 w = {cvtpk(a0[0], a0[1]), cvtpk(a0[2], a0[3]), cvtpk(a1[0], a1[1]), cvtpk(a1[2], a1[3])};
  *reinterpret_cast<u32x4*>(out + ((size_t)b * SQ + row) * (2 * DM) + blk * DM + nt * 160 + nb * 32 + g * 8) = w;
}

extern "C" void kernel_launch(void* const* d_in, const int* in_sizes, int n_in,
                              void* d_out, int out_size, void* d_ws, size_t ws_size,
                              hipStream_t stream) {
  const u16* xA  = (const u16*)d_in[0]; const u16* kA = (const u16*)d_in[1]; const u16* vA = (const u16*)d_in[2];
  const u16* wqA = (const u16*)d_in[3]; const u16* woA = (const u16*)d_in[4];
  const u16* xB  = (const u16*)d_in[5]; const u16* kB = (const u16*)d_in[6]; const u16* vB = (const u16*)d_in[7];
  const u16* wqB = (const u16*)d_in[8]; const u16* woB = (const u16*)d_in[9];
  constexpr size_t OWS_B = (size_t)2 * NBATCH * 256 * HD * 2;
  u16* ows = (u16*)d_ws;
  float* pws = (float*)((char*)d_ws + OWS_B);
  (void)hipFuncSetAttribute((const void*)pw::attn_pw, hipFuncAttributeMaxDynamicSharedMemorySize, 163840);
  pw::attn_pw<<<dim3(256), dim3(256), 163840, stream>>>(xA, kA, vA, wqA, xB, kB, vB, wqB, ows);
  (void)hipFuncSetAttribute((const void*)oproj_lc, hipFuncAttributeMaxDynamicSharedMemorySize, OL_SHM);
  oproj_lc<<<dim3(256), dim3(512), OL_SHM, stream>>>(ows, woA, woB, pws);
  oreduce2<<<dim3(2 * NBATCH * 4 * 40 * 128 / 256), dim3(256), 0, stream>>>(pws, (u16*)d_out);
}
```
